```python
import torch

def _softmax(x, axis=-1):
    return torch.softmax(x, dim=axis)
def _gelu(x):
    return torch.nn.functional.gelu(x, approximate="tanh")
def _silu(x):
    return torch.nn.functional.silu(x)
def _sigmoid(x):
    return torch.sigmoid(x)
def _layernorm(x, axis=-1, eps=1e-5):
    m = x.mean(dim=axis, keepdim=True)
    v = x.var(dim=axis, keepdim=True, unbiased=False)
    return (x - m) / torch.sqrt(v + eps)
def _scatter_add(tgt, idx, src, axis=0):
    return tgt.clone().index_add_(axis, idx, src)
def _segment_sum(src, seg, n):
    out = torch.zeros((n,) + src.shape[1:], dtype=src.dtype)
    return out.index_add_(0, seg, src)
def _pad(x, pw):
    flat = tuple(v for pair in reversed(pw) for v in pair)
    return torch.nn.functional.pad(x, flat)

kernel_name = 'concat_heads__gqa_block_cached__mha_block_cached__near_ridge'

def setup_inputs():

    _w = (lambda a: (lambda t: t.to(torch.bfloat16) if t.is_floating_point() else t)(torch.as_tensor(a)))
    def _A_setup():
        B, Sq, D = 16, 64, 512
        Hq, Hkv, Sk, Dh = 32, 8, 512, 128
        assert Hq % Hkv == 0, (Hq, Hkv)
        return {
            "x": _w((lambda *s: torch.randn(*s, dtype=torch.bfloat16) * 0.8)(B, Sq, D)),
            "k_cache": _w((lambda *s: torch.randn(*s, dtype=torch.bfloat16) * 0.8)(B, Hkv, Sk, Dh)),
            "v_cache": _w((lambda *s: torch.randn(*s, dtype=torch.bfloat16) * 0.05)(B, Hkv, Sk, Dh)),
            "wq": _w((lambda *s: torch.randn(*s, dtype=torch.bfloat16) * 0.8)(D, Hq * Dh) * (D ** -0.5)),
            "wo": _w((lambda *s: torch.randn(*s, dtype=torch.bfloat16) * 0.05)(Hq * Dh, D) * ((Hq * Dh) ** -0.5)),
        }
    _A_in = _A_setup()

    def _B_setup():
        B, Sq, D = 16, 64, 512
        H, Sk, Dh = 32, 512, 128
        return {
            "x": _w((lambda *s: torch.randn(*s, dtype=torch.bfloat16) * 0.8)(B, Sq, D)),
            "k_cache": _w((lambda *s: torch.randn(*s, dtype=torch.bfloat16) * 0.8)(B, H, Sk, Dh)),
            "v_cache": _w((lambda *s: torch.randn(*s, dtype=torch.bfloat16) * 0.05)(B, H, Sk, Dh)),
            "wq": _w((lambda *s: torch.randn(*s, dtype=torch.bfloat16) * 0.8)(D, H * Dh) * (D ** -0.5)),
            "wo": _w((lambda *s: torch.randn(*s, dtype=torch.bfloat16) * 0.05)(H * Dh, D) * ((H * Dh) ** -0.5)),
        }
    _B_in = _B_setup()

    return {
        "A_x": _A_in["x"],
        "A_k_cache": _A_in["k_cache"],
        "A_v_cache": _A_in["v_cache"],
        "A_wq": _A_in["wq"],
        "A_wo": _A_in["wo"],
        "B_x": _B_in["x"],
        "B_k_cache": _B_in["k_cache"],
        "B_v_cache": _B_in["v_cache"],
        "B_wq": _B_in["wq"],
        "B_wo": _B_in["wo"],
    }

def reference(A_x, A_k_cache, A_v_cache, A_wq, A_wo, B_x, B_k_cache, B_v_cache, B_wq, B_wo):
    def _A_ref(x, k_cache, v_cache, wq, wo):
        B, Sq, D = x.shape
        Hq, Dh = 32, 128
        Hkv = k_cache.shape[1]
        rep = Hq // Hkv
        q = (lambda x, *d: x.permute(*d))((x @ wq).reshape(B, Sq, Hq, Dh), 0, 2, 1, 3)
        k = (lambda x, n, axis: torch.repeat_interleave(x, n, dim=axis))(k_cache, rep, axis=1)
        v = (lambda x, n, axis: torch.repeat_interleave(x, n, dim=axis))(v_cache, rep, axis=1)
        scores = torch.einsum("bhqd,bhkd->bhqk", q, k) * (Dh ** -0.5)
        p = _softmax(scores, axis=-1)
        o = torch.einsum("bhqk,bhkd->bhqd", p, v)
        return (lambda x, *d: x.permute(*d))(o, 0, 2, 1, 3).reshape(B, Sq, Hq * Dh) @ wo

    def _B_ref(x, k_cache, v_cache, wq, wo):
        B, Sq, D = x.shape
        H, Dh = 32, 128
        q = (lambda x, *d: x.permute(*d))((x @ wq).reshape(B, Sq, H, Dh), 0, 2, 1, 3)
        scores = torch.einsum("bhqd,bhkd->bhqk", q, k_cache) * (Dh ** -0.5)
        p = _softmax(scores, axis=-1)
        o = torch.einsum("bhqk,bhkd->bhqd", p, v_cache)
        return (lambda x, *d: x.permute(*d))(o, 0, 2, 1, 3).reshape(B, Sq, H * Dh) @ wo

    a = _A_ref(A_x, A_k_cache, A_v_cache, A_wq, A_wo)
    b = _B_ref(B_x, B_k_cache, B_v_cache, B_wq, B_wo)
    return torch.concatenate([a, b], axis=-1)
```

```cpp
#include <hip/hip_runtime.h>
#include <hip/hip_bf16.h>
#include <stdint.h>

typedef unsigned short u16;
__device__ __forceinline__ float bf2f(u16 v) { return __uint_as_float(((unsigned)v) << 16); }
__device__ __forceinline__ u16 f2bf(float f) { __hip_bfloat16 b = __float2bfloat16(f); return *reinterpret_cast<u16*>(&b); }

__global__ void gemm_simple(const u16* __restrict__ A, const u16* __restrict__ B, u16* __restrict__ C, int M, int N, int K, int ldc) {
  __shared__ float As[16][65];
  __shared__ float Bs[16][65];
  int tx = threadIdx.x & 15, ty = threadIdx.x >> 4;
  int m0 = blockIdx.y * 64, n0 = blockIdx.x * 64;
  float acc[4][4] = {};
  for (int k0 = 0; k0 < K; k0 += 16) {
    for (int i = threadIdx.x; i < 16 * 64; i += 256) {
      int kk = i & 15, mm = i >> 4;
      As[kk][mm] = bf2f(A[(size_t)(m0 + mm) * K + k0 + kk]);
      int nn = i & 63, k2 = i >> 6;
      Bs[k2][nn] = bf2f(B[(size_t)(k0 + k2) * N + n0 + nn]);
    }
    __syncthreads();
    for (int kk = 0; kk < 16; ++kk) {
      float a[4], b[4];
      for (int i = 0; i < 4; ++i) a[i] = As[kk][ty * 4 + i];
      for (int j = 0; j < 4; ++j) b[j] = Bs[kk][tx * 4 + j];
      for (int i = 0; i < 4; ++i) for (int j = 0; j < 4; ++j) acc[i][j] += a[i] * b[j];
    }
    __syncthreads();
  }
  for (int i = 0; i < 4; ++i) for (int j = 0; j < 4; ++j)
    C[(size_t)(m0 + ty * 4 + i) * ldc + n0 + tx * 4 + j] = f2bf(acc[i][j]);
}

__global__ void attn_simple(const u16* __restrict__ Q, const u16* __restrict__ Kc, const u16* __restrict__ Vc, u16* __restrict__ O, int Hkv) {
  __shared__ float qs[128];
  __shared__ float ps[512];
  __shared__ float red[128];
  int q = blockIdx.x, h = blockIdx.y, b = blockIdx.z;
  int t = threadIdx.x;
  int hk = h / (32 / Hkv);
  qs[t] = bf2f(Q[((size_t)(b * 64 + q)) * 4096 + h * 128 + t]);
  __syncthreads();
  const u16* Kp = Kc + ((size_t)(b * Hkv + hk)) * 512 * 128;
  const u16* Vp = Vc + ((size_t)(b * Hkv + hk)) * 512 * 128;
  float mx = -1e30f;
  for (int k = t; k < 512; k += 128) {
    float s = 0;
    for (int d = 0; d < 128; ++d) s += qs[d] * bf2f(Kp[k * 128 + d]);
    s *= 0.08838834764831845f;
    ps[k] = s; mx = fmaxf(mx, s);
  }
  red[t] = mx; __syncthreads();
  for (int o = 64; o > 0; o >>= 1) { if (t < o) red[t] = fmaxf(red[t], red[t + o]); __syncthreads(); }
  mx = red[0]; __syncthreads();
  float sm = 0;
  for (int k = t; k < 512; k += 128) { float e = __expf(ps[k] - mx); ps[k] = e; sm += e; }
  red[t] = sm; __syncthreads();
  for (int o = 64; o > 0; o >>= 1) { if (t < o) red[t] += red[t + o]; __syncthreads(); }
  sm = red[0];
  float acc = 0;
  for (int k = 0; k < 512; ++k) acc += ps[k] * bf2f(Vp[k * 128 + t]);
  O[((size_t)(b * 64 + q)) * 4096 + h * 128 + t] = f2bf(acc / sm);
}

extern "C" void kernel_launch(void* const* d_in, const int* in_sizes, int n_in,
                              void* d_out, int out_size, void* d_ws, size_t ws_size,
                              hipStream_t stream) {
  u16* ws = (u16*)d_ws;
  for (int br = 0; br < 2; ++br) {
    const u16* x = (const u16*)d_in[br * 5 + 0];
    const u16* kc = (const u16*)d_in[br * 5 + 1];
    const u16* vc = (const u16*)d_in[br * 5 + 2];
    const u16* wq = (const u16*)d_in[br * 5 + 3];
    const u16* wo = (const u16*)d_in[br * 5 + 4];
    u16* q = ws + (size_t)br * 2 * 1024 * 4096;
    u16* o = q + (size_t)1024 * 4096;
    gemm_simple<<<dim3(4096 / 64, 1024 / 64), 256, 0, stream>>>(x, wq, q, 1024, 4096, 512, 4096);
    attn_simple<<<dim3(64, 32, 16), 128, 0, stream>>>(q, kc, vc, o, br == 0 ? 8 : 32);
    gemm_simple<<<dim3(512 / 64, 1024 / 64), 256, 0, stream>>>(o, wo, (u16*)d_out + br * 512, 1024, 512, 4096, 1024);
  }
}
```

```cpp
#include <hip/hip_runtime.h>
#include <stdint.h>

typedef __bf16 bf16x2 __attribute__((ext_vector_type(2)));
typedef __bf16 bf16x4 __attribute__((ext_vector_type(4)));
typedef __bf16 bf16x8 __attribute__((ext_vector_type(8)));
typedef float f32x2 __attribute__((ext_vector_type(2)));
typedef float f32x16 __attribute__((ext_vector_type(16)));
typedef unsigned u32x4 __attribute__((ext_vector_type(4)));
typedef int i32x4 __attribute__((ext_vector_type(4)));
#define LAS __attribute__((address_space(3)))
#define GAS __attribute__((address_space(1)))
#define DI __device__ __forceinline__

#define KV_AUX 2
#define W_AUX 0

constexpr int SPIN_LIMIT = 1 << 16;
constexpr int TILE = 16384;
constexpr int NSLOT = 9;
constexpr int RING = NSLOT * TILE;
constexpr int FULL_OFF = RING, FREE_OFF = RING + 64, XF_OFF = RING + 128;
constexpr int LDS_BYTES = RING + 192;

struct Params {
  const char* x[2]; const char* k[2]; const char* v[2]; const char* wq[2]; const char* wo[2];
  char* part;
};

DI unsigned rfl(unsigned x) { return __builtin_amdgcn_readfirstlane(x); }
DI unsigned cvtpk(float lo, float hi) { f32x2 v = {lo, hi}; bf16x2 b = __builtin_convertvector(v, bf16x2); return __builtin_bit_cast(unsigned, b); }
DI f32x16 mfma(bf16x8 a, bf16x8 b, f32x16 c) { return __builtin_amdgcn_mfma_f32_32x32x16_bf16(a, b, c, 0, 0, 0); }
DI bf16x8 trfrag(LAS char* p, int off0, int off1) {
  bf16x4 lo = __builtin_amdgcn_ds_read_tr16_b64_v4bf16((LAS bf16x4*)(p + off0));
  bf16x4 hi = __builtin_amdgcn_ds_read_tr16_b64_v4bf16((LAS bf16x4*)(p + off1));
  return __builtin_shufflevector(lo, hi, 0, 1, 2, 3, 4, 5, 6, 7);
}
DI bf16x8 pack8(const f32x16& x, int s, float sc) {
  u32x4 w = {cvtpk(x[8 * s + 0] * sc, x[8 * s + 1] * sc), cvtpk(x[8 * s + 2] * sc, x[8 * s + 3] * sc),
             cvtpk(x[8 * s + 4] * sc, x[8 * s + 5] * sc), cvtpk(x[8 * s + 6] * sc, x[8 * s + 7] * sc)};
  return __builtin_bit_cast(bf16x8, w);
}
DI bf16x8 pack8n(const f32x16& x, int s) {
  u32x4 w = {cvtpk(x[8 * s + 0], x[8 * s + 1]), cvtpk(x[8 * s + 2], x[8 * s + 3]), cvtpk(x[8 * s + 4], x[8 * s + 5]), cvtpk(x[8 * s + 6], x[8 * s + 7])};
  return __builtin_bit_cast(bf16x8, w);
}
DI int min4(i32x4 v) { return min(min(v[0], v[1]), min(v[2], v[3])); }
DI int slot_add(int s, int n) { s += n * TILE; return s >= RING ? s - RING : s; }

#define STAMP(i) do {} while (0)

__global__ __launch_bounds__(512, 2) void fused_kernel(Params p) {
  extern __shared__ __attribute__((aligned(16))) char lds[];
  LAS char* L = (LAS char*)lds;
  const int tid = threadIdx.x, lane = tid & 63;
  const int wave = rfl(tid >> 6);
  const int bid = blockIdx.x;
  const int xcd = bid & 7, rr = bid >> 3;
  const int b = rr >> 1, hp = xcd * 2 + (rr & 1);
  const int first = b & 1;
  const int rot = (b >> 1) & 3;
  volatile LAS int* FULLW = (volatile LAS int*)(L + FULL_OFF);
  volatile LAS int* FREEW = (volatile LAS int*)(L + FREE_OFF);
  volatile LAS int* XFW = (volatile LAS int*)(L + XF_OFF);
  if (tid < 48) ((LAS int*)(L + RING))[tid] = 0;
  STAMP(0);
  __syncthreads();

  if (wave >= 4) {
    const int lw = wave - 4;
    const unsigned nrow = 4 * lw + (lane >> 4), nch = ((lane & 15) ^ nrow) * 16;
    const unsigned trow = 8 * (lw >> 1) + ((lane >> 2) & 7), tcol = 128 * (lw & 1) + 64 * (lane >> 5) + 16 * (lane & 3);
    const unsigned offK = nrow * 256 + nch, offX = nrow * 1024 + nch;
    const unsigned offV = trow * 256 + tcol, offWQ = trow * 8192 + tcol, offWO = trow * 1024 + tcol;
    int idx = 0, slot = 0, freed = 0, prev = 0;
    auto acquire = [&]() {
      const int need = idx + 1 - NSLOT;
      if (freed < need) {
        freed = rfl(min4(*(volatile LAS i32x4*)FREEW));
        if (freed < need) {
          asm volatile("s_waitcnt vmcnt(0)" ::: "memory");
          if (lane == 0) FULLW[lw] = idx;
          prev = idx;
          int sp = 0;
          do { __builtin_amdgcn_s_sleep(1); freed = rfl(min4(*(volatile LAS i32x4*)FREEW)); if (++sp > SPIN_LIMIT) freed = 1 << 30; } while (freed < need);
        }
      }
    };
    const unsigned ldsw = (unsigned)(uintptr_t)L + lw * 1024;
#define DMA1(ldsa, voff, gbase, AUX) do { \
      if ((AUX) == 2) asm volatile("s_mov_b32 m0, %0\n\ts_nop 0\n\tglobal_load_lds_dwordx4 %1, %2 nt" :: "s"(ldsa), "v"(voff), "s"(gbase) : "memory"); \
      else            asm volatile("s_mov_b32 m0, %0\n\ts_nop 0\n\tglobal_load_lds_dwordx4 %1, %2" :: "s"(ldsa), "v"(voff), "s"(gbase) : "memory"); } while (0)
#define ISSUE(base, voff, step16, AUX) do { acquire(); \
      _Pragma("unroll") for (int i_ = 0; i_ < 4; ++i_) DMA1(ldsw + slot + i_ * 4096, voff, (base) + (size_t)(i_ * (step16)), AUX); \
      asm volatile("s_waitcnt vmcnt(8)" ::: "memory"); \
      if (lane == 0) FULLW[lw] = prev; \
      prev = idx; ++idx; slot = slot_add(slot, 1); } while (0)
#pragma unroll 1
    for (int r = 0; r < 2; ++r) {
      const int br = first ^ r;
      const GAS char* xb = (const GAS char*)p.x[br] + (size_t)b * 65536;
      const GAS char* wq = (const GAS char*)p.wq[br] + hp * 512;
      const GAS char* wo = (const GAS char*)p.wo[br] + (size_t)hp * 256 * 1024;
#pragma unroll 1
      for (int j = 0; j < 4; ++j) ISSUE(xb + ((j + rot) & 3) * 256, offX, 16 * 1024, W_AUX);
#pragma unroll 1
      for (int j = 0; j < 8; ++j) {
        const GAS char* wj = wq + (size_t)(((j + 2 * rot) & 7) * 64) * 8192;
        ISSUE(wj, offWQ, 16 * 8192, W_AUX);
        ISSUE(wj + 256, offWQ, 16 * 8192, W_AUX);
      }
      if (br == 1) {
        const GAS char* k0 = (const GAS char*)p.k[1] + (size_t)(b * 32 + 2 * hp) * 131072;
        const GAS char* v0 = (const GAS char*)p.v[1] + (size_t)(b * 32 + 2 * hp) * 131072;
        ISSUE(k0, offK, 4096, KV_AUX); ISSUE(k0 + 131072, offK, 4096, KV_AUX);
#pragma unroll 1
        for (int t = 0; t < 8; ++t) {
          if (t < 7) { ISSUE(k0 + (t + 1) * TILE, offK, 4096, KV_AUX); ISSUE(k0 + 131072 + (t + 1) * TILE, offK, 4096, KV_AUX); }
          ISSUE(v0 + t * TILE, offV, 4096, KV_AUX); ISSUE(v0 + 131072 + t * TILE, offV, 4096, KV_AUX);
        }
      } else {
        const GAS char* k0 = (const GAS char*)p.k[0] + (size_t)(b * 8 + (hp >> 1)) * 131072;
        const GAS char* v0 = (const GAS char*)p.v[0] + (size_t)(b * 8 + (hp >> 1)) * 131072;
        ISSUE(k0, offK, 4096, KV_AUX);
#pragma unroll 1
        for (int t = 0; t < 8; ++t) {
          if (t < 7) ISSUE(k0 + (t + 1) * TILE, offK, 4096, KV_AUX);
          ISSUE(v0 + t * TILE, offV, 4096, KV_AUX);
        }
      }
      acquire(); ++idx; slot = slot_add(slot, 1);
      acquire(); ++idx; slot = slot_add(slot, 1);
#pragma unroll 1
      for (int ps = 0; ps < 4; ++ps)
#pragma unroll 1
        for (int kk = 0; kk < 4; ++kk) ISSUE(wo + (size_t)kk * 65536 + ((ps + rot) & 3) * 256, offWO, 16 * 1024, W_AUX);
      STAMP(1 + r);
    }
    asm volatile("s_waitcnt vmcnt(0)" ::: "memory");
    if (lane == 0) FULLW[lw] = idx;
    STAMP(3);
    return;
  }

  const int hh = wave >> 1, qh = wave & 1;
  const int r32 = lane & 31, h = lane >> 5;
  int avail = 0, idx = 0, slot = 0;
  auto wait_full = [&](int need) {
    int sp = 0;
    while (avail < need) { avail = rfl(min4(*(volatile LAS i32x4*)FULLW)); if (avail < need) { __builtin_amdgcn_s_sleep(1); if (++sp > SPIN_LIMIT) avail = 1 << 30; } }
    asm volatile("" ::: "memory");
  };
  auto release = [&](int n) {
    asm volatile("s_waitcnt lgkmcnt(0)" ::: "memory");
    if (lane == 0) FREEW[wave] = n;
  };
  const int nbase = 256 * r32 + 16 * (h ^ (r32 & 15));
  const int pq = (lane & 3), pr = ((pq & 1) << 1) | (pq >> 1);
  const int tb_acc = ((lane & 3) << 3) | (((lane >> 2) & 3) << 6) | (((lane >> 4) & 1) << 5) | (h << 8);
  const int tb_wq = (pr << 3) | (((lane >> 2) & 3) << 6) | (((lane >> 4) & 1) << 5) | (h << 11);

#pragma unroll 1
  for (int r = 0; r < 2; ++r) {
    const int br = first ^ r;
    bf16x8 qf[8];
    {
      bf16x8 xf[32];
#pragma unroll
      for (int j = 0; j < 4; ++j) {
        wait_full(idx + j + 1);
        const int sb = slot_add(slot, j) + qh * 8192;
#pragma unroll
        for (int s = 0; s < 8; ++s) xf[j * 8 + s] = *(LAS bf16x8*)(L + (nbase ^ (sb + 32 * s)));
      }
      idx += 4; slot = slot_add(slot, 4);
      release(idx);
      f32x16 qa[4] = {};
#pragma unroll
      for (int j = 0; j < 8; ++j) {
        wait_full(idx + hh + 1);
        LAS char* tp = L + slot_add(slot, hh) + tb_wq;
#pragma unroll
        for (int ks = 0; ks < 4; ++ks)
#pragma unroll
          for (int db = 0; db < 4; ++db) qa[db] = mfma(trfrag(tp, 4096 * ks + 512 * db, 4096 * ks + 512 * db + 256), xf[j * 4 + ks], qa[db]);
        idx += 2; slot = slot_add(slot, 2);
        release(idx);
      }
      const float C = 0.12751743082f;
#pragma unroll
      for (int db = 0; db < 4; ++db) { qf[2 * db] = pack8(qa[db], 0, C); qf[2 * db + 1] = pack8(qa[db], 1, C); }
    }
    STAMP(1 + 4 * r);
    const int nh = br ? 2 : 1, ho = br ? hh : 0;
    f32x16 oa[4] = {};
    float m = -__builtin_inff(), l = 0.f;
    f32x16 s0, s1;
    auto qk = [&](int sb, f32x16& a0, f32x16& a1) {
      a0 = f32x16{}; a1 = f32x16{};
#pragma unroll
      for (int s = 0; s < 8; ++s) {
        LAS char* kp = L + (nbase ^ (sb + 32 * s));
        a0 = mfma(*(LAS bf16x8*)kp, qf[s], a0);
        a1 = mfma(*(LAS bf16x8*)(kp + 8192), qf[s], a1);
      }
    };
    wait_full(idx + nh);
    qk(slot_add(slot, ho), s0, s1);
    idx += nh; slot = slot_add(slot, nh);
    release(idx);
    for (int t = 0; t < 8; ++t) {
      const int ng = (t < 7) ? 2 * nh : nh;
      wait_full(idx + ng);
      const int ksb = slot_add(slot, ho), vsb = slot_add(slot, ng - nh + ho);
      float mx = fmaxf(s0[0], s1[0]);
#pragma unroll
      for (int i = 1; i < 16; ++i) mx = fmaxf(mx, fmaxf(s0[i], s1[i]));
      { auto sw = __builtin_amdgcn_permlane32_swap(__float_as_uint(mx), __float_as_uint(mx), false, false); mx = fmaxf(__uint_as_float(sw[0]), __uint_as_float(sw[1])); }
      const float mn = fmaxf(m, mx);
      const float alpha = __builtin_amdgcn_exp2f(m - mn);
      m = mn;
      f32x16 n0, n1;
      if (t < 7) qk(ksb, n0, n1);
      float ps = 0.f;
#pragma unroll
      for (int i = 0; i < 16; ++i) { s0[i] = __builtin_amdgcn_exp2f(s0[i] - mn); s1[i] = __builtin_amdgcn_exp2f(s1[i] - mn); ps += s0[i] + s1[i]; }
      l = l * alpha + ps;
      bf16x8 pf[4] = {pack8n(s0, 0), pack8n(s0, 1), pack8n(s1, 0), pack8n(s1, 1)};
#pragma unroll
      for (int db = 0; db < 4; ++db)
#pragma unroll
        for (int i = 0; i < 16; ++i) oa[db][i] *= alpha;
      LAS char* vp = L + vsb + tb_acc;
#pragma unroll
      for (int ks = 0; ks < 4; ++ks)
#pragma unroll
        for (int db = 0; db < 4; ++db) oa[db] = mfma(trfrag(vp, 4096 * ks + 512 * db, 4096 * ks + 512 * db + 2048), pf[ks], oa[db]);
      idx += ng; slot = slot_add(slot, ng);
      release(idx);
      s0 = n0; s1 = n1;
    }
    STAMP(2 + 4 * r);
    { auto sw = __builtin_amdgcn_permlane32_swap(__float_as_uint(l), __float_as_uint(l), false, false); l = __uint_as_float(sw[0]) + __uint_as_float(sw[1]); }
    const float il = 1.f / l;
    wait_full(idx + 2);
    {
      LAS char* xp = L + slot_add(slot, hh) + qh * 8192 + lane * 16;
#pragma unroll
      for (int db = 0; db < 4; ++db) { *(LAS bf16x8*)(xp + 2048 * db) = pack8(oa[db], 0, il); *(LAS bf16x8*)(xp + 2048 * db + 1024) = pack8(oa[db], 1, il); }
    }
    asm volatile("s_waitcnt lgkmcnt(0)" ::: "memory");
    if (lane == 0) XFW[wave] = r + 1;
    { int sp = 0; while (rfl(min4(*(volatile LAS i32x4*)XFW)) < r + 1 && ++sp < SPIN_LIMIT) __builtin_amdgcn_s_sleep(1); }
    asm volatile("" ::: "memory");
    bf16x8 of[32];
#pragma unroll
    for (int g = 0; g < 2; ++g) {
      LAS char* xp = L + slot_add(slot, g) + lane * 16;
#pragma unroll
      for (int i = 0; i < 16; ++i) of[g * 16 + i] = *(LAS bf16x8*)(xp + 1024 * i);
    }
    idx += 2; slot = slot_add(slot, 2);
    release(idx);
    STAMP(3 + 4 * r);
    GAS char* pb = (GAS char*)p.part + ((size_t)((br * 16 + b) * 16 + hp)) * 65536 + wave * 4096 + lane * 16;
#pragma unroll 1
    for (int ps = 0; ps < 4; ++ps) {
      f32x16 c0 = {}, c1 = {};
#pragma unroll
      for (int kk = 0; kk < 4; ++kk) {
        wait_full(idx + 1);
        LAS char* wp = L + slot + tb_acc + 512 * wave;
#pragma unroll
        for (int ks = 0; ks < 4; ++ks) {
          const bf16x8 a = trfrag(wp, 4096 * ks, 4096 * ks + 2048);
          c0 = mfma(a, of[(kk >> 1) * 16 + (kk & 1) * 4 + ks], c0);
          c1 = mfma(a, of[(kk >> 1) * 16 + 8 + (kk & 1) * 4 + ks], c1);
        }
        idx += 1; slot = slot_add(slot, 1);
        release(idx);
      }
      GAS char* pp = pb + ((ps + rot) & 3) * 16384;
      *(GAS bf16x8*)(pp) = pack8n(c0, 0); *(GAS bf16x8*)(pp + 1024) = pack8n(c0, 1);
      *(GAS bf16x8*)(pp + 2048) = pack8n(c1, 0); *(GAS bf16x8*)(pp + 3072) = pack8n(c1, 1);
    }
    STAMP(4 + 4 * r);
  }
}

__global__ __launch_bounds__(256) void reduce_kernel(const char* __restrict__ part, char* __restrict__ out) {
  const int lane = threadIdx.x & 63, gw = blockIdx.x * 4 + (threadIdx.x >> 6);
  const int qb = gw & 1, u = (gw >> 1) & 15, bb = gw >> 5;
  const char* src = part + (size_t)bb * 16 * 65536 + u * 4096 + qb * 2048 + lane * 16;
  u32x4 v[16][2];
#pragma unroll
  for (int g = 0; g < 16; ++g) { v[g][0] = *(const u32x4*)(src + (size_t)g * 65536); v[g][1] = *(const u32x4*)(src + (size_t)g * 65536 + 1024); }
  float a[16];
#pragma unroll
  for (int i = 0; i < 16; ++i) a[i] = 0.f;
#pragma unroll
  for (int g = 0; g < 16; ++g)
#pragma unroll
    for (int i = 0; i < 2; ++i)
#pragma unroll
      for (int e = 0; e < 4; ++e) { a[8 * i + 2 * e] += __uint_as_float(v[g][i][e] << 16); a[8 * i + 2 * e + 1] += __uint_as_float(v[g][i][e] & 0xffff0000u); }
  const int br = bb >> 4, b = bb & 15, q = 32 * qb + (lane & 31), h = lane >> 5;
  char* op = out + ((size_t)(b * 64 + q) * 1024 + br * 512 + u * 32) * 2 + h * 16;
#pragma unroll
  for (int i = 0; i < 2; ++i) {
    unsigned x0 = cvtpk(a[8 * i + 0], a[8 * i + 1]), x1 = cvtpk(a[8 * i + 2], a[8 * i + 3]);
    unsigned y0 = cvtpk(a[8 * i + 4], a[8 * i + 5]), y1 = cvtpk(a[8 * i + 6], a[8 * i + 7]);
    auto r0 = __builtin_amdgcn_permlane32_swap(x0, y0, false, false);
    auto r1 = __builtin_amdgcn_permlane32_swap(x1, y1, false, false);
    u32x4 w = {r0[0], r1[0], r0[1], r1[1]};
    *(u32x4*)(op + 32 * i) = w;
  }
}

extern "C" void kernel_launch(void* const* d_in, const int* in_sizes, int n_in, void* d_out, int out_size, void* d_ws, size_t ws_size, hipStream_t stream) {
  Params p;
  for (int br = 0; br < 2; ++br) {
    p.x[br] = (const char*)d_in[br * 5 + 0]; p.k[br] = (const char*)d_in[br * 5 + 1]; p.v[br] = (const char*)d_in[br * 5 + 2];
    p.wq[br] = (const char*)d_in[br * 5 + 3]; p.wo[br] = (const char*)d_in[br * 5 + 4];
  }
  p.part = (char*)d_ws;
  (void)hipFuncSetAttribute((const void*)fused_kernel, hipFuncAttributeMaxDynamicSharedMemorySize, LDS_BYTES);
  hipLaunchKernelGGL(fused_kernel, dim3(256), dim3(512), LDS_BYTES, stream, p);
  hipLaunchKernelGGL(reduce_kernel, dim3(256), dim3(256), 0, stream, (const char*)d_ws, (char*)d_out);
}
```

```cpp
#include <hip/hip_runtime.h>
#include <stdint.h>

typedef __bf16 bf16x2 __attribute__((ext_vector_type(2)));
typedef __bf16 bf16x4 __attribute__((ext_vector_type(4)));
typedef __bf16 bf16x8 __attribute__((ext_vector_type(8)));
typedef float f32x2 __attribute__((ext_vector_type(2)));
typedef float f32x16 __attribute__((ext_vector_type(16)));
typedef unsigned u32x4 __attribute__((ext_vector_type(4)));
typedef int i32x4 __attribute__((ext_vector_type(4)));
#define LAS __attribute__((address_space(3)))
#define GAS __attribute__((address_space(1)))
#define DI __device__ __forceinline__
#define SB() __builtin_amdgcn_sched_barrier(0)

#define KV_AUX 2
#define W_AUX 0

constexpr int SPIN_LIMIT = 1 << 16;
constexpr int TILE = 16384;
constexpr int NSLOT = 9;
constexpr int RING = NSLOT * TILE;
constexpr int FULL_OFF = RING, FREE_OFF = RING + 64, XF_OFF = RING + 128;
constexpr int LDS_BYTES = RING + 192;
constexpr int NQ = 36;

struct Params {
  const char* x[2]; const char* k[2]; const char* v[2]; const char* wq[2]; const char* wo[2];
  char* part;
};

DI unsigned rfl(unsigned x) { return __builtin_amdgcn_readfirstlane(x); }
DI unsigned cvtpk(float lo, float hi) { f32x2 v = {lo, hi}; bf16x2 b = __builtin_convertvector(v, bf16x2); return __builtin_bit_cast(unsigned, b); }
DI f32x16 mfma(bf16x8 a, bf16x8 b, f32x16 c) { return __builtin_amdgcn_mfma_f32_32x32x16_bf16(a, b, c, 0, 0, 0); }
DI bf16x8 trfrag(LAS char* p, int off0, int off1) {
  bf16x4 lo = __builtin_amdgcn_ds_read_tr16_b64_v4bf16((LAS bf16x4*)(p + off0));
  bf16x4 hi = __builtin_amdgcn_ds_read_tr16_b64_v4bf16((LAS bf16x4*)(p + off1));
  return __builtin_shufflevector(lo, hi, 0, 1, 2, 3, 4, 5, 6, 7);
}
DI bf16x8 pack8(const f32x16& x, int s, float sc) {
  u32x4 w = {cvtpk(x[8 * s + 0] * sc, x[8 * s + 1] * sc), cvtpk(x[8 * s + 2] * sc, x[8 * s + 3] * sc),
             cvtpk(x[8 * s + 4] * sc, x[8 * s + 5] * sc), cvtpk(x[8 * s + 6] * sc, x[8 * s + 7] * sc)};
  return __builtin_bit_cast(bf16x8, w);
}
DI bf16x8 pack8n(const f32x16& x, int s) {
  u32x4 w = {cvtpk(x[8 * s + 0], x[8 * s + 1]), cvtpk(x[8 * s + 2], x[8 * s + 3]), cvtpk(x[8 * s + 4], x[8 * s + 5]), cvtpk(x[8 * s + 6], x[8 * s + 7])};
  return __builtin_bit_cast(bf16x8, w);
}
DI float max3f(float a, float b, float c) { return __builtin_fmaxf(__builtin_fmaxf(a, b), c); }
DI int min4(i32x4 v) { return min(min(v[0], v[1]), min(v[2], v[3])); }
DI int slot_add(int s, int n) { s += n * TILE; return s >= RING ? s - RING : s; }

#define STAMP(i) do {} while (0)

__global__ __launch_bounds__(768, 3) void fused_kernel(Params p) {
  extern __shared__ __attribute__((aligned(16))) char lds[];
  LAS char* L = (LAS char*)lds;
  const int tid = threadIdx.x, lane = tid & 63;
  const int wave = rfl(tid >> 6);
  const int bid = blockIdx.x;
  const int g = bid & 7, rr = bid >> 3;
  const int br = rr & 1, b = rr >> 1;
  const int rot = b & 3;
  const int nh = br ? 4 : 1;
  const int NKV = 16 * nh;
  volatile LAS int* FULLW = (volatile LAS int*)(L + FULL_OFF);
  volatile LAS int* FREEW = (volatile LAS int*)(L + FREE_OFF);
  volatile LAS int* XFW = (volatile LAS int*)(L + XF_OFF);
  if (tid < 48) ((LAS int*)(L + RING))[tid] = 0;
  STAMP(0);
  __syncthreads();
  auto min8 = [&](volatile LAS int* w) { i32x4 a = *(volatile LAS i32x4*)w, c = *(volatile LAS i32x4*)(w + 4); return (int)rfl(min(min4(a), min4(c))); };

  if (wave >= 8) {
    const int lw = wave - 8;
    const unsigned nrow = 4 * lw + (lane >> 4), nch = ((lane & 15) ^ nrow) * 16;
    const unsigned trow = 8 * (lw >> 1) + ((lane >> 2) & 7), tcol = 128 * (lw & 1) + 64 * (lane >> 5) + 16 * (lane & 3);
    const unsigned offK = nrow * 256 + nch, offX = nrow * 1024 + nch;
    const unsigned offV = trow * 256 + tcol, offWQ = trow * 8192 + tcol, offWO = trow * 1024 + tcol;
    int idx = 0, slot = 0, freed = 0;
    auto acquire = [&](int need) {
      if (freed < need) {
        freed = min8(FREEW);
        if (freed < need) {
          asm volatile("s_waitcnt vmcnt(0)" ::: "memory");
          if (lane == 0) FULLW[lw] = idx;
          int sp = 0;
          do { __builtin_amdgcn_s_sleep(1); freed = min8(FREEW); if (++sp > SPIN_LIMIT) freed = 1 << 30; } while (freed < need);
        }
      }
    };
    const unsigned ldsw = (unsigned)(uintptr_t)L + lw * 1024;
    int pub = 0;
#define DMA1(ldsa, voff, gbase, AUX) do { \
      if ((AUX) == 2) asm volatile("s_mov_b32 m0, %0\n\ts_nop 0\n\tglobal_load_lds_dwordx4 %1, %2 nt" :: "s"(ldsa), "v"(voff), "s"(gbase) : "memory"); \
      else            asm volatile("s_mov_b32 m0, %0\n\ts_nop 0\n\tglobal_load_lds_dwordx4 %1, %2" :: "s"(ldsa), "v"(voff), "s"(gbase) : "memory"); } while (0)
#define PUBLISH() do { asm volatile("s_waitcnt vmcnt(8)" ::: "memory"); \
      if (lane == 0) __hip_atomic_fetch_max((LAS int*)&FULLW[lw], idx - 1, __ATOMIC_RELAXED, __HIP_MEMORY_SCOPE_WORKGROUP); ++idx; } while (0)
#define ISSUE(base, voff, step16, AUX) do { \
      _Pragma("unroll") for (int i_ = 0; i_ < 4; ++i_) DMA1(ldsw + slot + i_ * 4096, voff, (base) + (size_t)(i_ * (step16)), AUX); \
      PUBLISH(); } while (0)
    (void)pub;
    const GAS char* xb = (const GAS char*)p.x[br] + (size_t)b * 65536;
    const GAS char* wq = (const GAS char*)p.wq[br] + g * 1024;
    const GAS char* wo = (const GAS char*)p.wo[br] + (size_t)g * 512 * 1024;
#pragma unroll 1
    for (int j = 0; j < 4; ++j) {
      const int c = (j + rot) & 3;
      acquire(idx + 1 - NSLOT); ISSUE(xb + c * 256, offX, 16 * 1024, W_AUX); slot = slot_add(slot, 1);
#pragma unroll 1
      for (int i = 0; i < 8; ++i) {
        acquire(idx + 1 - NSLOT); ISSUE(wq + (size_t)(c * 128 + (i >> 2) * 64) * 8192 + (i & 3) * 256, offWQ, 16 * 8192, W_AUX); slot = slot_add(slot, 1);
      }
    }
    STAMP(1);
    {
      const size_t hb = (size_t)(br ? b * 32 + 4 * g : b * 8 + g) * 131072;
      const GAS char* k0 = (const GAS char*)p.k[br] + hb;
      const GAS char* v0 = (const GAS char*)p.v[br] + hb;
#pragma unroll 1
      for (int t = 0; t < 16; ++t)
#pragma unroll 1
        for (int hh = 0; hh < nh; ++hh) {
          acquire(idx + 1 - NSLOT);
          const size_t o = (size_t)hh * 131072 + t * 8192;
          DMA1(ldsw + slot, offK, k0 + o, KV_AUX); DMA1(ldsw + slot + 4096, offK, k0 + o + 4096, KV_AUX);
          DMA1(ldsw + slot + 8192, offV, v0 + o, KV_AUX); DMA1(ldsw + slot + 12288, offV, v0 + o + 4096, KV_AUX);
          PUBLISH(); slot = slot_add(slot, 1);
        }
    }
    STAMP(2);
    {
      const int s0 = slot; int pos = 0;
#pragma unroll 1
      for (int i = 0; i < 32; ++i) {
        const int kk = ((i >> 2) + 2 * rot) & 7;
        acquire(i < 5 ? idx + 1 - NSLOT : idx - 4);
        slot = slot_add(s0, pos);
        ISSUE(wo + (size_t)kk * 65536 + (i & 3) * 256, offWO, 16 * 1024, W_AUX);
        pos = pos == 4 ? 0 : pos + 1;
      }
    }
    asm volatile("s_waitcnt vmcnt(0)" ::: "memory");
    if (lane == 0) FULLW[lw] = idx;
    STAMP(3);
    return;
  }

  const int hd = wave >> 1, qh = wave & 1;
  const int r32 = lane & 31, h = lane >> 5;
  int avail = 0;
  auto wait_full = [&](int need) {
    int sp = 0;
    while (avail < need) { avail = rfl(min4(*(volatile LAS i32x4*)FULLW)); if (avail < need) { __builtin_amdgcn_s_sleep(1); if (++sp > SPIN_LIMIT) avail = 1 << 30; } }
    asm volatile("" ::: "memory");
  };
  auto release = [&](int n) {
    asm volatile("s_waitcnt lgkmcnt(0)" ::: "memory");
    if (lane == 0) FREEW[wave] = n;
  };
  const int nbase = 256 * r32 + 16 * (h ^ (r32 & 15));
  const int pq = (lane & 3), pr = ((pq & 1) << 1) | (pq >> 1);
  const int tb_acc = ((lane & 3) << 3) | (((lane >> 2) & 3) << 6) | (((lane >> 4) & 1) << 5) | (h << 8);
  const int tb_wq = (pr << 3) | (((lane >> 2) & 3) << 6) | (((lane >> 4) & 1) << 5) | (h << 11);

  bf16x8 qf[8];
  {
    f32x16 qa[4] = {};
#pragma unroll 1
    for (int j = 0; j < 4; ++j) {
      const int i0 = 9 * j;
      bf16x8 xf[8];
      wait_full(i0 + 1);
#pragma unroll
      for (int s = 0; s < 8; ++s) xf[s] = *(LAS bf16x8*)(L + (nbase ^ (32 * s)) + qh * 8192);
      release(i0 + 1 + hd);
#pragma unroll
      for (int u = 0; u < 2; ++u) {
        wait_full(i0 + 2 + 4 * u + hd);
        LAS char* tp = L + (1 + 4 * u + hd) * TILE + tb_wq;
#pragma unroll
        for (int ks = 0; ks < 4; ++ks)
#pragma unroll
          for (int db = 0; db < 4; ++db) qa[db] = mfma(trfrag(tp, 4096 * ks + 512 * db, 4096 * ks + 512 * db + 256), xf[u * 4 + ks], qa[db]);
        release(u == 0 ? i0 + 5 + hd : i0 + 9);
      }
    }
    const float C = 0.12751743082f;
#pragma unroll
    for (int db = 0; db < 4; ++db) { qf[2 * db] = pack8(qa[db], 0, C); qf[2 * db + 1] = pack8(qa[db], 1, C); }
  }
  STAMP(1);
  f32x16 oa[4] = {};
  float m = -__builtin_inff(), l = 0.f;
  const int ho = br ? hd : 0;
  int idx = NQ + ho, slot = ho * TILE;
  release(idx);
#pragma unroll 1
  for (int t = 0; t < 16; ++t) {
    wait_full(idx + 1);
    bf16x8 kf[8];
#pragma unroll
    for (int s = 0; s < 8; ++s) kf[s] = *(LAS bf16x8*)(L + (nbase ^ (slot + 32 * s)));
    f32x16 s0 = {};
#pragma unroll
    for (int s = 0; s < 8; ++s) s0 = mfma(kf[s], qf[s], s0);
    SB();
    LAS char* vp = L + slot + 8192 + tb_acc;
    bf16x8 vf[8];
#pragma unroll
    for (int i = 0; i < 8; ++i) vf[i] = trfrag(vp, 4096 * (i >> 2) + 512 * (i & 3), 4096 * (i >> 2) + 512 * (i & 3) + 2048);
    SB();
    float mx = max3f(s0[0], s0[1], s0[2]);
#pragma unroll
    for (int i = 3; i < 15; i += 2) mx = max3f(mx, s0[i], s0[i + 1]);
    mx = max3f(mx, s0[15], m);
    { auto sw = __builtin_amdgcn_permlane32_swap(__float_as_uint(mx), __float_as_uint(mx), false, false); mx = fmaxf(__uint_as_float(sw[0]), __uint_as_float(sw[1])); }
    const float alpha = __builtin_amdgcn_exp2f(m - mx);
    m = mx;
    float ps = 0.f;
#pragma unroll
    for (int i = 0; i < 16; ++i) { s0[i] = __builtin_amdgcn_exp2f(s0[i] - mx); ps += s0[i]; }
    l = l * alpha + ps;
    const bf16x8 pf0 = pack8n(s0, 0), pf1 = pack8n(s0, 1);
    idx += nh;
    release(idx);
#pragma unroll
    for (int db = 0; db < 4; ++db) {
#pragma unroll
      for (int i = 0; i < 16; ++i) oa[db][i] *= alpha;
      oa[db] = mfma(vf[db], pf0, oa[db]);
    }
#pragma unroll
    for (int db = 0; db < 4; ++db) oa[db] = mfma(vf[4 + db], pf1, oa[db]);
    slot = slot_add(slot, nh);
  }
  STAMP(2);
  { auto sw = __builtin_amdgcn_permlane32_swap(__float_as_uint(l), __float_as_uint(l), false, false); l = __uint_as_float(sw[0]) + __uint_as_float(sw[1]); }
  const float il = 1.f / l;
  idx = NQ + NKV;
  const int s0 = br ? TILE * (64 % NSLOT) : TILE * (16 % NSLOT);
  { int sp = 0; while (min8(FREEW) < idx && ++sp < SPIN_LIMIT) __builtin_amdgcn_s_sleep(1); }
  asm volatile("" ::: "memory");
  {
    LAS char* xp = L + slot_add(s0, 5 + hd) + qh * 8192 + lane * 16;
#pragma unroll
    for (int db = 0; db < 4; ++db) { *(LAS bf16x8*)(xp + 2048 * db) = pack8(oa[db], 0, il); *(LAS bf16x8*)(xp + 2048 * db + 1024) = pack8(oa[db], 1, il); }
  }
  asm volatile("s_waitcnt lgkmcnt(0)" ::: "memory");
  if (lane == 0) XFW[wave] = 1;
  { int sp = 0; while (min8(XFW) < 1 && ++sp < SPIN_LIMIT) __builtin_amdgcn_s_sleep(1); }
  asm volatile("" ::: "memory");
  STAMP(3);
  f32x16 c00 = {}, c01 = {}, c10 = {}, c11 = {};
  {
    int pos = hd;
    idx += hd;
    release(idx);
#pragma unroll 1
    for (int ii = 0; ii < 8; ++ii) {
      const int kk = (ii + 2 * rot) & 7;
      wait_full(idx + 1);
      LAS char* wp = L + slot_add(s0, pos) + tb_acc + 1024 * qh;
      LAS char* op = L + slot_add(s0, 5 + (kk >> 1)) + (kk & 1) * 4096 + lane * 16;
#pragma unroll
      for (int ks = 0; ks < 4; ++ks) {
        const bf16x8 a0 = trfrag(wp, 4096 * ks, 4096 * ks + 2048), a1 = trfrag(wp, 4096 * ks + 512, 4096 * ks + 512 + 2048);
        const bf16x8 b0 = *(LAS bf16x8*)(op + 1024 * ks), b1 = *(LAS bf16x8*)(op + 8192 + 1024 * ks);
        c00 = mfma(a0, b0, c00); c01 = mfma(a0, b1, c01); c10 = mfma(a1, b0, c10); c11 = mfma(a1, b1, c11);
      }
      idx += 4;
      release(idx);
      pos = pos == 0 ? 4 : pos - 1;
    }
  }
  GAS char* pp = (GAS char*)p.part + ((size_t)((br * 16 + b) * 8 + g)) * 65536 + wave * 8192 + lane * 16;
  *(GAS bf16x8*)(pp) = pack8n(c00, 0); *(GAS bf16x8*)(pp + 1024) = pack8n(c00, 1);
  *(GAS bf16x8*)(pp + 2048) = pack8n(c01, 0); *(GAS bf16x8*)(pp + 3072) = pack8n(c01, 1);
  *(GAS bf16x8*)(pp + 4096) = pack8n(c10, 0); *(GAS bf16x8*)(pp + 5120) = pack8n(c10, 1);
  *(GAS bf16x8*)(pp + 6144) = pack8n(c11, 0); *(GAS bf16x8*)(pp + 7168) = pack8n(c11, 1);
  STAMP(4);
}

__global__ __launch_bounds__(256) void reduce_kernel(const char* __restrict__ part, char* __restrict__ out) {
  const int lane = threadIdx.x & 63, gw = blockIdx.x * 4 + (threadIdx.x >> 6);
  const int qb = gw & 1, u = (gw >> 1) & 15, bb = gw >> 5;
  const char* src = part + (size_t)bb * 8 * 65536 + u * 4096 + qb * 2048 + lane * 16;
  u32x4 v[8][2];
#pragma unroll
  for (int g = 0; g < 8; ++g) { v[g][0] = *(const u32x4*)(src + (size_t)g * 65536); v[g][1] = *(const u32x4*)(src + (size_t)g * 65536 + 1024); }
  float a[16];
#pragma unroll
  for (int i = 0; i < 16; ++i) a[i] = 0.f;
#pragma unroll
  for (int g = 0; g < 8; ++g)
#pragma unroll
    for (int i = 0; i < 2; ++i)
#pragma unroll
      for (int e = 0; e < 4; ++e) { a[8 * i + 2 * e] += __uint_as_float(v[g][i][e] << 16); a[8 * i + 2 * e + 1] += __uint_as_float(v[g][i][e] & 0xffff0000u); }
  const int br = bb >> 4, b = bb & 15, q = 32 * qb + (lane & 31), h = lane >> 5;
  char* op = out + ((size_t)(b * 64 + q) * 1024 + br * 512 + u * 32) * 2 + h * 16;
#pragma unroll
  for (int i = 0; i < 2; ++i) {
    unsigned x0 = cvtpk(a[8 * i + 0], a[8 * i + 1]), x1 = cvtpk(a[8 * i + 2], a[8 * i + 3]);
    unsigned y0 = cvtpk(a[8 * i + 4], a[8 * i + 5]), y1 = cvtpk(a[8 * i + 6], a[8 * i + 7]);
    auto r0 = __builtin_amdgcn_permlane32_swap(x0, y0, false, false);
    auto r1 = __builtin_amdgcn_permlane32_swap(x1, y1, false, false);
    u32x4 w = {r0[0], r1[0], r0[1], r1[1]};
    *(u32x4*)(op + 32 * i) = w;
  }
}

extern "C" void kernel_launch(void* const* d_in, const int* in_sizes, int n_in, void* d_out, int out_size, void* d_ws, size_t ws_size, hipStream_t stream) {
  Params p;
  for (int br = 0; br < 2; ++br) {
    p.x[br] = (const char*)d_in[br * 5 + 0]; p.k[br] = (const char*)d_in[br * 5 + 1]; p.v[br] = (const char*)d_in[br * 5 + 2];
    p.wq[br] = (const char*)d_in[br * 5 + 3]; p.wo[br] = (const char*)d_in[br * 5 + 4];
  }
  p.part = (char*)d_ws;
  (void)hipFuncSetAttribute((const void*)fused_kernel, hipFuncAttributeMaxDynamicSharedMemorySize, LDS_BYTES);
  hipLaunchKernelGGL(fused_kernel, dim3(256), dim3(768), LDS_BYTES, stream, p);
  hipLaunchKernelGGL(reduce_kernel, dim3(256), dim3(256), 0, stream, (const char*)d_ws, (char*)d_out);
}
```

```cpp
#define PAUX 2
#include <hip/hip_runtime.h>
#include <stdint.h>

typedef __bf16 bf16x2 __attribute__((ext_vector_type(2)));
typedef __bf16 bf16x4 __attribute__((ext_vector_type(4)));
typedef __bf16 bf16x8 __attribute__((ext_vector_type(8)));
typedef float f32x2 __attribute__((ext_vector_type(2)));
typedef float f32x16 __attribute__((ext_vector_type(16)));
typedef unsigned u32x4 __attribute__((ext_vector_type(4)));
typedef int i32x4 __attribute__((ext_vector_type(4)));
#define LAS __attribute__((address_space(3)))
#define GAS __attribute__((address_space(1)))
#define DI __device__ __forceinline__
#define SB() __builtin_amdgcn_sched_barrier(0)

#define KV_AUX 2
#define W_AUX 0

constexpr int SPIN_LIMIT = 1 << 16;
constexpr int TILE = 16384;
constexpr int NSLOT = 9;
constexpr int RING = NSLOT * TILE;
constexpr int FULL_OFF = RING, FREE_OFF = RING + 64, XF_OFF = RING + 128;
constexpr int LDS_BYTES = RING + 192;
constexpr int NQ = 36;

struct Params {
  const char* x[2]; const char* k[2]; const char* v[2]; const char* wq[2]; const char* wo[2];
  char* part;
};

DI unsigned rfl(unsigned x) { return __builtin_amdgcn_readfirstlane(x); }
DI unsigned cvtpk(float lo, float hi) { f32x2 v = {lo, hi}; bf16x2 b = __builtin_convertvector(v, bf16x2); return __builtin_bit_cast(unsigned, b); }
DI f32x16 mfma(bf16x8 a, bf16x8 b, f32x16 c) { return __builtin_amdgcn_mfma_f32_32x32x16_bf16(a, b, c, 0, 0, 0); }
DI bf16x8 trfrag(LAS char* p, int off0, int off1) {
  bf16x4 lo = __builtin_amdgcn_ds_read_tr16_b64_v4bf16((LAS bf16x4*)(p + off0));
  bf16x4 hi = __builtin_amdgcn_ds_read_tr16_b64_v4bf16((LAS bf16x4*)(p + off1));
  return __builtin_shufflevector(lo, hi, 0, 1, 2, 3, 4, 5, 6, 7);
}
DI bf16x8 pack8(const f32x16& x, int s, float sc) {
  u32x4 w = {cvtpk(x[8 * s + 0] * sc, x[8 * s + 1] * sc), cvtpk(x[8 * s + 2] * sc, x[8 * s + 3] * sc),
             cvtpk(x[8 * s + 4] * sc, x[8 * s + 5] * sc), cvtpk(x[8 * s + 6] * sc, x[8 * s + 7] * sc)};
  return __builtin_bit_cast(bf16x8, w);
}
DI bf16x8 pack8n(const f32x16& x, int s) {
  u32x4 w = {cvtpk(x[8 * s + 0], x[8 * s + 1]), cvtpk(x[8 * s + 2], x[8 * s + 3]), cvtpk(x[8 * s + 4], x[8 * s + 5]), cvtpk(x[8 * s + 6], x[8 * s + 7])};
  return __builtin_bit_cast(bf16x8, w);
}
DI float max3f(float a, float b, float c) { return __builtin_fmaxf(__builtin_fmaxf(a, b), c); }
DI int min4(i32x4 v) { return min(min(v[0], v[1]), min(v[2], v[3])); }
DI int slot_add(int s, int n) { s += n * TILE; return s >= RING ? s - RING : s; }

#define STAMP(i) do {} while (0)

__global__ __launch_bounds__(768, 3) void fused_kernel(Params p) {
  extern __shared__ __attribute__((aligned(16))) char lds[];
  LAS char* L = (LAS char*)lds;
  const int tid = threadIdx.x, lane = tid & 63;
  const int wave = rfl(tid >> 6);
  const int bid = blockIdx.x;
  const int g = bid & 7, rr = bid >> 3;
  const int br = rr & 1, b = rr >> 1;
  const int rot = b & 3;
  const int nh = br ? 4 : 1;
  const int NKV = 16 * nh;
  volatile LAS int* FULLW = (volatile LAS int*)(L + FULL_OFF);
  volatile LAS int* FREEW = (volatile LAS int*)(L + FREE_OFF);
  volatile LAS int* XFW = (volatile LAS int*)(L + XF_OFF);
  if (tid < 48) ((LAS int*)(L + RING))[tid] = 0;
  STAMP(0);
  __syncthreads();
  auto min8 = [&](volatile LAS int* w) { i32x4 a = *(volatile LAS i32x4*)w, c = *(volatile LAS i32x4*)(w + 4); return (int)rfl(min(min4(a), min4(c))); };

  if (wave >= 8) {
    const int lw = wave - 8;
    const unsigned nrow = 4 * lw + (lane >> 4), nch = ((lane & 15) ^ nrow) * 16;
    const unsigned trow = 8 * (lw >> 1) + ((lane >> 2) & 7), tcol = 128 * (lw & 1) + 64 * (lane >> 5) + 16 * (lane & 3);
    const unsigned offK = nrow * 256 + nch, offX = nrow * 1024 + nch;
    const unsigned offV = trow * 256 + tcol, offWQ = trow * 8192 + tcol, offWO = trow * 1024 + tcol;
    int idx = 0, slot = 0, freed = 0;
    auto acquire = [&](int need) {
      if (freed < need) {
        freed = min8(FREEW);
        if (freed < need) {
          asm volatile("s_waitcnt vmcnt(0)" ::: "memory");
          if (lane == 0) FULLW[lw] = idx;
          int sp = 0;
          do { __builtin_amdgcn_s_sleep(1); freed = min8(FREEW); if (++sp > SPIN_LIMIT) freed = 1 << 30; } while (freed < need);
        }
      }
    };
    const unsigned ldsw = (unsigned)(uintptr_t)L + lw * 1024;
    int pub = 0;
#define DMA1(ldsa, voff, gbase, AUX) do { \
      if ((AUX) == 2) asm volatile("s_mov_b32 m0, %0\n\ts_nop 0\n\tglobal_load_lds_dwordx4 %1, %2 nt" :: "s"(ldsa), "v"(voff), "s"(gbase) : "memory"); \
      else            asm volatile("s_mov_b32 m0, %0\n\ts_nop 0\n\tglobal_load_lds_dwordx4 %1, %2" :: "s"(ldsa), "v"(voff), "s"(gbase) : "memory"); } while (0)
#define PUBLISH() do { asm volatile("s_waitcnt vmcnt(8)" ::: "memory"); \
      if (lane == 0) __hip_atomic_fetch_max((LAS int*)&FULLW[lw], idx - 1, __ATOMIC_RELAXED, __HIP_MEMORY_SCOPE_WORKGROUP); ++idx; } while (0)
#define ISSUE(base, voff, step16, AUX) do { \
      _Pragma("unroll") for (int i_ = 0; i_ < 4; ++i_) DMA1(ldsw + slot + i_ * 4096, voff, (base) + (size_t)(i_ * (step16)), AUX); \
      PUBLISH(); } while (0)
    (void)pub;
    const GAS char* xb = (const GAS char*)p.x[br] + (size_t)b * 65536;
    const GAS char* wq = (const GAS char*)p.wq[br] + g * 1024;
    const GAS char* wo = (const GAS char*)p.wo[br] + (size_t)g * 512 * 1024;
#pragma unroll 1
    for (int j = 0; j < 4; ++j) {
      const int c = (j + rot) & 3;
      acquire(idx + 1 - NSLOT); ISSUE(xb + c * 256, offX, 16 * 1024, W_AUX); slot = slot_add(slot, 1);
#pragma unroll 1
      for (int i = 0; i < 8; ++i) {
        acquire(idx + 1 - NSLOT); ISSUE(wq + (size_t)(c * 128 + (i >> 2) * 64) * 8192 + (i & 3) * 256, offWQ, 16 * 8192, W_AUX); slot = slot_add(slot, 1);
      }
    }
    STAMP(1);
    {
      const size_t hb = (size_t)(br ? b * 32 + 4 * g : b * 8 + g) * 131072;
      const GAS char* k0 = (const GAS char*)p.k[br] + hb;
      const GAS char* v0 = (const GAS char*)p.v[br] + hb;
#pragma unroll 1
      for (int t = 0; t < 16; ++t)
#pragma unroll 1
        for (int hh = 0; hh < nh; ++hh) {
          acquire(idx + 1 - NSLOT);
          const size_t o = (size_t)hh * 131072 + t * 8192;
          DMA1(ldsw + slot, offK, k0 + o, KV_AUX); DMA1(ldsw + slot + 4096, offK, k0 + o + 4096, KV_AUX);
          DMA1(ldsw + slot + 8192, offV, v0 + o, KV_AUX); DMA1(ldsw + slot + 12288, offV, v0 + o + 4096, KV_AUX);
          PUBLISH(); slot = slot_add(slot, 1);
        }
    }
    STAMP(2);
    {
      const int s0 = slot; int pos = 0;
#pragma unroll 1
      for (int i = 0; i < 32; ++i) {
        const int kk = ((i >> 2) + 2 * rot) & 7;
        acquire(i < 5 ? idx + 1 - NSLOT : idx - 4);
        slot = slot_add(s0, pos);
        ISSUE(wo + (size_t)kk * 65536 + (i & 3) * 256, offWO, 16 * 1024, W_AUX);
        pos = pos == 4 ? 0 : pos + 1;
      }
    }
    asm volatile("s_waitcnt vmcnt(0)" ::: "memory");
    if (lane == 0) FULLW[lw] = idx;
    STAMP(3);
    return;
  }

  const int hd = wave >> 1, qh = wave & 1;
  const int r32 = lane & 31, h = lane >> 5;
  int avail = 0;
  auto wait_full = [&](int need) {
    int sp = 0;
    while (avail < need) { avail = rfl(min4(*(volatile LAS i32x4*)FULLW)); if (avail < need) { __builtin_amdgcn_s_sleep(1); if (++sp > SPIN_LIMIT) avail = 1 << 30; } }
    asm volatile("" ::: "memory");
  };
  auto release = [&](int n) {
    asm volatile("s_waitcnt lgkmcnt(0)" ::: "memory");
    if (lane == 0) FREEW[wave] = n;
  };
  const int nbase = 256 * r32 + 16 * (h ^ (r32 & 15));
  const int pq = (lane & 3), pr = ((pq & 1) << 1) | (pq >> 1);
  const int tb_acc = ((lane & 3) << 3) | (((lane >> 2) & 3) << 6) | (((lane >> 4) & 1) << 5) | (h << 8);
  const int tb_wq = (pr << 3) | (((lane >> 2) & 3) << 6) | (((lane >> 4) & 1) << 5) | (h << 11);

  bf16x8 qf[8];
  {
    f32x16 qa[4] = {};
#pragma unroll 1
    for (int j = 0; j < 4; ++j) {
      const int i0 = 9 * j;
      bf16x8 xf[8];
      wait_full(i0 + 1);
#pragma unroll
      for (int s = 0; s < 8; ++s) xf[s] = *(LAS bf16x8*)(L + (nbase ^ (32 * s)) + qh * 8192);
      release(i0 + 1 + hd);
#pragma unroll
      for (int u = 0; u < 2; ++u) {
        wait_full(i0 + 2 + 4 * u + hd);
        LAS char* tp = L + (1 + 4 * u + hd) * TILE + tb_wq;
#pragma unroll
        for (int ks = 0; ks < 4; ++ks)
#pragma unroll
          for (int db = 0; db < 4; ++db) qa[db] = mfma(trfrag(tp, 4096 * ks + 512 * db, 4096 * ks + 512 * db + 256), xf[u * 4 + ks], qa[db]);
        release(u == 0 ? i0 + 5 + hd : i0 + 9);
      }
    }
    const float C = 0.12751743082f;
#pragma unroll
    for (int db = 0; db < 4; ++db) { qf[2 * db] = pack8(qa[db], 0, C); qf[2 * db + 1] = pack8(qa[db], 1, C); }
  }
  STAMP(1);
  f32x16 oa[4] = {};
  float m = -__builtin_inff(), l = 0.f;
  const int ho = br ? hd : 0;
  int idx = NQ + ho, slot = ho * TILE;
  release(idx);
#pragma unroll 1
  for (int t = 0; t < 16; ++t) {
    wait_full(idx + 1);
    bf16x8 kf[8];
#pragma unroll
    for (int s = 0; s < 8; ++s) kf[s] = *(LAS bf16x8*)(L + (nbase ^ (slot + 32 * s)));
    f32x16 s0 = {};
#pragma unroll
    for (int s = 0; s < 8; ++s) s0 = mfma(kf[s], qf[s], s0);
    SB();
    LAS char* vp = L + slot + 8192 + tb_acc;
    bf16x8 vf[8];
#pragma unroll
    for (int i = 0; i < 8; ++i) vf[i] = trfrag(vp, 4096 * (i >> 2) + 512 * (i & 3), 4096 * (i >> 2) + 512 * (i & 3) + 2048);
    SB();
    float mx = max3f(s0[0], s0[1], s0[2]);
#pragma unroll
    for (int i = 3; i < 15; i += 2) mx = max3f(mx, s0[i], s0[i + 1]);
    mx = max3f(mx, s0[15], m);
    { auto sw = __builtin_amdgcn_permlane32_swap(__float_as_uint(mx), __float_as_uint(mx), false, false); mx = fmaxf(__uint_as_float(sw[0]), __uint_as_float(sw[1])); }
    const float alpha = __builtin_amdgcn_exp2f(m - mx);
    m = mx;
    float ps = 0.f;
#pragma unroll
    for (int i = 0; i < 16; ++i) { s0[i] = __builtin_amdgcn_exp2f(s0[i] - mx); ps += s0[i]; }
    l = l * alpha + ps;
    const bf16x8 pf0 = pack8n(s0, 0), pf1 = pack8n(s0, 1);
    idx += nh;
    release(idx);
#pragma unroll
    for (int db = 0; db < 4; ++db) {
#pragma unroll
      for (int i = 0; i < 16; ++i) oa[db][i] *= alpha;
      oa[db] = mfma(vf[db], pf0, oa[db]);
    }
#pragma unroll
    for (int db = 0; db < 4; ++db) oa[db] = mfma(vf[4 + db], pf1, oa[db]);
    slot = slot_add(slot, nh);
  }
  STAMP(2);
  { auto sw = __builtin_amdgcn_permlane32_swap(__float_as_uint(l), __float_as_uint(l), false, false); l = __uint_as_float(sw[0]) + __uint_as_float(sw[1]); }
  const float il = 1.f / l;
  idx = NQ + NKV;
  const int s0 = br ? TILE * (64 % NSLOT) : TILE * (16 % NSLOT);
  { int sp = 0; while (min8(FREEW) < idx && ++sp < SPIN_LIMIT) __builtin_amdgcn_s_sleep(1); }
  asm volatile("" ::: "memory");
  {
    LAS char* xp = L + slot_add(s0, 5 + hd) + qh * 8192 + lane * 16;
#pragma unroll
    for (int db = 0; db < 4; ++db) { *(LAS bf16x8*)(xp + 2048 * db) = pack8(oa[db], 0, il); *(LAS bf16x8*)(xp + 2048 * db + 1024) = pack8(oa[db], 1, il); }
  }
  asm volatile("s_waitcnt lgkmcnt(0)" ::: "memory");
  if (lane == 0) XFW[wave] = 1;
  { int sp = 0; while (min8(XFW) < 1 && ++sp < SPIN_LIMIT) __builtin_amdgcn_s_sleep(1); }
  asm volatile("" ::: "memory");
  STAMP(3);
  f32x16 c00 = {}, c01 = {}, c10 = {}, c11 = {};
  {
    int pos = hd;
    idx += hd;
    release(idx);
#pragma unroll 1
    for (int ii = 0; ii < 8; ++ii) {
      const int kk = (ii + 2 * rot) & 7;
      wait_full(idx + 1);
      LAS char* wp = L + slot_add(s0, pos) + tb_acc + 1024 * qh;
      LAS char* op = L + slot_add(s0, 5 + (kk >> 1)) + (kk & 1) * 4096 + lane * 16;
#pragma unroll
      for (int ks = 0; ks < 4; ++ks) {
        const bf16x8 a0 = trfrag(wp, 4096 * ks, 4096 * ks + 2048), a1 = trfrag(wp, 4096 * ks + 512, 4096 * ks + 512 + 2048);
        const bf16x8 b0 = *(LAS bf16x8*)(op + 1024 * ks), b1 = *(LAS bf16x8*)(op + 8192 + 1024 * ks);
        c00 = mfma(a0, b0, c00); c01 = mfma(a0, b1, c01); c10 = mfma(a1, b0, c10); c11 = mfma(a1, b1, c11);
      }
      idx += 4;
      release(idx);
      pos = pos == 0 ? 4 : pos - 1;
    }
  }
  GAS char* pp = (GAS char*)p.part + ((size_t)((br * 16 + b) * 8 + g)) * 65536 + wave * 8192 + lane * 16;
  *(GAS bf16x8*)(pp) = pack8n(c00, 0); *(GAS bf16x8*)(pp + 1024) = pack8n(c00, 1);
  *(GAS bf16x8*)(pp + 2048) = pack8n(c01, 0); *(GAS bf16x8*)(pp + 3072) = pack8n(c01, 1);
  *(GAS bf16x8*)(pp + 4096) = pack8n(c10, 0); *(GAS bf16x8*)(pp + 5120) = pack8n(c10, 1);
  *(GAS bf16x8*)(pp + 6144) = pack8n(c11, 0); *(GAS bf16x8*)(pp + 7168) = pack8n(c11, 1);
  STAMP(4);
}

__global__ __launch_bounds__(256) void reduce_kernel(const char* __restrict__ part, char* __restrict__ out) {
  const int lane = threadIdx.x & 63, gw = blockIdx.x * 4 + (threadIdx.x >> 6);
  const int qb = gw & 1, u = (gw >> 1) & 15, bb = gw >> 5;
  const char* src = part + (size_t)bb * 8 * 65536 + u * 4096 + qb * 2048 + lane * 16;
  u32x4 v[8][2];
#pragma unroll
  for (int g = 0; g < 8; ++g) { v[g][0] = *(const u32x4*)(src + (size_t)g * 65536); v[g][1] = *(const u32x4*)(src + (size_t)g * 65536 + 1024); }
  float a[16];
#pragma unroll
  for (int i = 0; i < 16; ++i) a[i] = 0.f;
#pragma unroll
  for (int g = 0; g < 8; ++g)
#pragma unroll
    for (int i = 0; i < 2; ++i)
#pragma unroll
      for (int e = 0; e < 4; ++e) { a[8 * i + 2 * e] += __uint_as_float(v[g][i][e] << 16); a[8 * i + 2 * e + 1] += __uint_as_float(v[g][i][e] & 0xffff0000u); }
  const int br = bb >> 4, b = bb & 15, q = 32 * qb + (lane & 31), h = lane >> 5;
  char* op = out + ((size_t)(b * 64 + q) * 1024 + br * 512 + u * 32) * 2 + h * 16;
#pragma unroll
  for (int i = 0; i < 2; ++i) {
    unsigned x0 = cvtpk(a[8 * i + 0], a[8 * i + 1]), x1 = cvtpk(a[8 * i + 2], a[8 * i + 3]);
    unsigned y0 = cvtpk(a[8 * i + 4], a[8 * i + 5]), y1 = cvtpk(a[8 * i + 6], a[8 * i + 7]);
    auto r0 = __builtin_amdgcn_permlane32_swap(x0, y0, false, false);
    auto r1 = __builtin_amdgcn_permlane32_swap(x1, y1, false, false);
    u32x4 w = {r0[0], r1[0], r0[1], r1[1]};
    *(u32x4*)(op + 32 * i) = w;
  }
}

struct Probe { const char* p[4]; unsigned n[4]; };
__global__ __launch_bounds__(256) void probe_kernel(Probe q) {
  extern __shared__ __attribute__((aligned(16))) char lds[];
  const int wave = rfl(threadIdx.x >> 6), lane = threadIdx.x & 63;
  int slot = 0;
  for (int r = 0; r < 4; ++r) {
    const unsigned per = q.n[r] / 256;
    const char* src = q.p[r] + (size_t)blockIdx.x * per;
    for (unsigned o = wave * 4096; o < per; o += 16384) {
#pragma unroll
      for (int j = 0; j < 4; ++j) __builtin_amdgcn_global_load_lds((const GAS unsigned*)(src + o + j * 1024 + lane * 16), (LAS unsigned*)((LAS char*)lds + wave * 16384 + slot * 4096 + j * 1024), 16, 0, PAUX);
      asm volatile("s_waitcnt vmcnt(8)" ::: "memory");
      slot = (slot + 1) & 3;
    }
  }
  asm volatile("s_waitcnt vmcnt(0)" ::: "memory");
}
extern "C" void kernel_launch(void* const* d_in, const int* in_sizes, int n_in, void* d_out, int out_size, void* d_ws, size_t ws_size, hipStream_t stream) {
  Params p;
  for (int br = 0; br < 2; ++br) {
    p.x[br] = (const char*)d_in[br * 5 + 0]; p.k[br] = (const char*)d_in[br * 5 + 1]; p.v[br] = (const char*)d_in[br * 5 + 2];
    p.wq[br] = (const char*)d_in[br * 5 + 3]; p.wo[br] = (const char*)d_in[br * 5 + 4];
  }
  p.part = (char*)d_ws;
  (void)hipFuncSetAttribute((const void*)fused_kernel, hipFuncAttributeMaxDynamicSharedMemorySize, LDS_BYTES);
  Probe q; for (int i = 0; i < 4; ++i) { q.p[i] = (const char*)d_in[1]; q.n[i] = 0; }
  (void)hipFuncSetAttribute((const void*)probe_kernel, hipFuncAttributeMaxDynamicSharedMemorySize, 98304);
  hipLaunchKernelGGL(probe_kernel, dim3(256), dim3(256), 98304, stream, q);
  hipLaunchKernelGGL(fused_kernel, dim3(256), dim3(768), LDS_BYTES, stream, p);
  hipLaunchKernelGGL(reduce_kernel, dim3(256), dim3(256), 0, stream, (const char*)d_ws, (char*)d_out);
}
```

```cpp
#include <hip/hip_runtime.h>
#include <stdint.h>

typedef __bf16 bf16x2 __attribute__((ext_vector_type(2)));
typedef float f32x2 __attribute__((ext_vector_type(2)));
typedef float f32x4 __attribute__((ext_vector_type(4)));
typedef short s16x4 __attribute__((ext_vector_type(4)));
typedef unsigned u32x2 __attribute__((ext_vector_type(2)));
typedef unsigned u32x4 __attribute__((ext_vector_type(4)));
typedef uint64_t u64x2 __attribute__((ext_vector_type(2)));
typedef unsigned u32x8 __attribute__((ext_vector_type(8)));
#define LAS __attribute__((address_space(3)))
#define DI __device__ __forceinline__

#define KVNT " nt"
#define PSC " sc1"
#define OSC " sc1"
#define RSC " sc1"
#define LPRIO 2
#define PST 81920
#define WST 20480
#define WNT ""
#define VARB(b) ((b >> 2) & 3)
#define PSLEEP 4
#define KMA 0xf623866
#define KMB 0xf51f52e

constexpr int LDS_BYTES = 163840, NVAR = 5;

struct Params {
  const char* x[2]; const char* k[2]; const char* v[2]; const char* wq[2]; const char* wo[2];
  char* part; char* out;
  unsigned long long* stamps;
};

__device__ const unsigned sched_tab[NVAR * 512] = {
  0x128, 0x38, 0x4038, 0x2000208, 0x3040008, 0x4040208, 0x5080008, 0x6080208, 0x50011828, 0x70c0008, 0x580c0208, 0x9000100, 0x1100008, 0x50002828, 0x2100208, 0x40002828,
  0x140008, 0x3140208, 0x4180008, 0x40002828, 0x5180208, 0x61c0008, 0x40002828, 0x71c0208, 0x8000200, 0x40002828, 0x1200008, 0x2200208, 0x40002828, 0x240008, 0x3240208, 0x9280008,
  0x40002828, 0x4280208, 0x52c0008, 0x40002828, 0x62c0208, 0x7000300, 0x40002828, 0x1300008, 0x2300208, 0x40002828, 0x340008, 0x3340208, 0x8380008, 0x40002828, 0x4380208, 0x93c0008,
  0x40002828, 0x53c0208, 0x30002828, 0x26000010, 0x20000028, 0x21000410, 0x2000110, 0x20005c28, 0x128, 0x510, 0x20004c28, 0x3001010, 0x20005c28, 0x7001410, 0x20005c28, 0x20,
  0x4001110, 0x20005c28, 0x8001510, 0x20005c28, 0x5002010, 0x20005c28, 0x9002410, 0x30, 0x30005c28, 0x6002110, 0x30005c28, 0x1002510, 0x4030, 0x30005c28, 0x2003010, 0x48,
  0x40005c28, 0x3410, 0x30005c28, 0x20, 0x20, 0x20, 0x20, 0x20, 0x20, 0x20, 0x20, 0x20, 0x20, 0x20, 0x20, 0x20,
  0x20, 0x20, 0x20, 0x20, 0x20, 0x20, 0x20, 0x20, 0x20, 0x20, 0x20, 0x20, 0x20, 0x20, 0x20, 0x20,
  0x20, 0x20, 0x20, 0x20, 0x20, 0x20, 0x20, 0x20, 0x20, 0x20, 0x20, 0x20, 0x20, 0x20, 0x20, 0x20,
  0x3003110, 0x20005c28, 0x7003510, 0x6000018, 0x30005c28, 0x1000218, 0x2008018, 0x40005c28, 0x50008218, 0x53010018, 0x47010218, 0x128, 0x6018018, 0x30001a28, 0x1018218, 0x2020018,
  0x30002a28, 0x20218, 0x3028018, 0x30002a28, 0x4028218, 0x6030018, 0x30002a28, 0x7030218, 0x1038018, 0x30002a28, 0x2038218, 0x40018, 0x30002a28, 0x3048018, 0x4050018, 0x30002a28,
  0x6058018, 0x1060018, 0x30002a28, 0x7068018, 0x8070018, 0x30002a28, 0x78018, 0x2040218, 0x30002a28, 0x3048218, 0x4050218, 0x30002a28, 0x1058218, 0x6060218, 0x30002a28, 0x7068218,
  0x8070218, 0x30002a28, 0x78218, 0x20002a28, 0x10002a28, 0x40, 0x40, 0x40, 0x40, 0x40, 0x40, 0x40, 0x40, 0x40, 0x40, 0x40,
  0x100000, 0x204000, 0x14c000, 0x254000, 0x1c000, 0x1a4000, 0x44000, 0x80000, 0x10000, 0xd8000, 0x1e0000, 0x184000, 0x40000, 0xa4000, 0x14000, 0xdc000,
  0x104000, 0x180000, 0x1e0000, 0x64000, 0x80000, 0x0, 0xc0000, 0x118000, 0x184000, 0x48000, 0x1c0000, 0x20c000, 0x1c000, 0x90000, 0xe0000, 0x114000,
  0x64000, 0x198000, 0x1c4000, 0x208000, 0x0, 0xc000, 0x1c000, 0x0, 0x0, 0x0, 0x0, 0x0, 0x0, 0x0, 0x0, 0x0,
  0x0, 0x0, 0x0, 0x0, 0x0, 0x0, 0x0, 0x0, 0x0, 0x0, 0x0, 0x0, 0x0, 0x0, 0x0, 0x0,
  0x0, 0x0, 0x0, 0x0, 0x0, 0x0, 0x0, 0x0, 0x0, 0x0, 0x0, 0x0, 0x0, 0x0, 0x0, 0x0,
  0x100000, 0x204000, 0x14c000, 0x254000, 0x1c000, 0x1a4000, 0x44000, 0x80000, 0x10000, 0xd8000, 0x1e0000, 0x184000, 0x40000, 0xa4000, 0x14000, 0xdc000,
  0x104000, 0x180000, 0x1e0000, 0x64000, 0x80000, 0x0, 0xc0000, 0x118000, 0x184000, 0x48000, 0x1c0000, 0x20c000, 0x1c000, 0x90000, 0xe0000, 0x114000,
  0x64000, 0x198000, 0x1c4000, 0x208000, 0x0, 0xc000, 0x1c000, 0x0, 0x0, 0x0, 0x0, 0x0, 0x0, 0x0, 0x0, 0x0,
  0x0, 0x0, 0x0, 0x0, 0x0, 0x0, 0x0, 0x0, 0x0, 0x0, 0x0, 0x0, 0x0, 0x0, 0x0, 0x0,
  0x0, 0x0, 0x0, 0x0, 0x0, 0x0, 0x0, 0x0, 0x0, 0x0, 0x0, 0x0, 0x0, 0x0, 0x0, 0x0,
  0x100000, 0x208000, 0x150000, 0x258000, 0x20000, 0x1a4000, 0x48000, 0x8c000, 0x14000, 0xdc000, 0x1e0000, 0x188000, 0x4c000, 0x90000, 0x18000, 0xdc000,
  0x108000, 0x18c000, 0x1d0000, 0x54000, 0x80000, 0x0, 0xc0000, 0x118000, 0x184000, 0x48000, 0x1c0000, 0x20c000, 0x1c000, 0x90000, 0xe0000, 0x114000,
  0x64000, 0x198000, 0x1c4000, 0x208000, 0x0, 0xc000, 0x1c000, 0x0, 0x0, 0x0, 0x0, 0x0, 0x0, 0x0, 0x0, 0x0,
  0x0, 0x0, 0x0, 0x0, 0x0, 0x0, 0x0, 0x0, 0x0, 0x0, 0x0, 0x0, 0x0, 0x0, 0x0, 0x0,
  0x0, 0x0, 0x0, 0x0, 0x0, 0x0, 0x0, 0x0, 0x0, 0x0, 0x0, 0x0, 0x0, 0x0, 0x0, 0x0,
  0x100000, 0x208000, 0x150000, 0x258000, 0x20000, 0x1a4000, 0x48000, 0x8c000, 0x14000, 0xdc000, 0x1e0000, 0x188000, 0x4c000, 0x90000, 0x18000, 0xdc000,
  0x108000, 0x18c000, 0x1d0000, 0x54000, 0x80000, 0x0, 0xc0000, 0x118000, 0x184000, 0x48000, 0x1c0000, 0x20c000, 0x1c000, 0x90000, 0xe0000, 0x114000,
  0x64000, 0x198000, 0x1c4000, 0x208000, 0x0, 0xc000, 0x1c000, 0x0, 0x0, 0x0, 0x0, 0x0, 0x0, 0x0, 0x0, 0x0,
  0x0, 0x0, 0x0, 0x0, 0x0, 0x0, 0x0, 0x0, 0x0, 0x0, 0x0, 0x0, 0x0, 0x0, 0x0, 0x0,
  0x0, 0x0, 0x0, 0x0, 0x0, 0x0, 0x0, 0x0, 0x0, 0x0, 0x0, 0x0, 0x0, 0x0, 0x0, 0x0,
  0x2040008, 0x3080008, 0x40c0008, 0x55000100, 0x6100008, 0x57140008, 0x8180008, 0x91c0008, 0x51000200, 0x200008, 0x52240008, 0x3280008, 0x442c0008, 0x5000300, 0x6300008, 0x57340008,
  0x8380008, 0x493c0008, 0x10, 0x41020410, 0x2000410, 0x53020010, 0x4000110, 0x55020510, 0x6000510, 0x57020110, 0x8001010, 0x59021410, 0x1410, 0x51021010, 0x2000000, 0x53000208,
  0x4040208, 0x55080208, 0x60c0208, 0x57000100, 0x8100208, 0x59140208, 0x180208, 0x11c0208, 0x53000200, 0x2200208, 0x54240208, 0x5280208, 0x462c0208, 0x7000300, 0x8300208, 0x59340208,
  0x380208, 0x413c0208, 0x2001110, 0x43021510, 0x4001510, 0x55021110, 0x6002010, 0x57022410, 0x8002410, 0x59022010, 0x2110, 0x51022510, 0x2002510, 0x53022110, 0x20, 0x20,
  0x4003010, 0x55023410, 0x6003410, 0x57023010, 0x8003110, 0x59023510, 0x3510, 0x51023110, 0x4040110, 0x55060510, 0x6000018, 0x57000218, 0x8008018, 0x59008218, 0x50010018, 0x1010218,
  0x54018018, 0x5018218, 0x56020018, 0x7020218, 0x58028018, 0x28218, 0x59030018, 0x1030218, 0x52038018, 0x4038218, 0x55040018, 0x6040510, 0x57060110, 0x8040010, 0x50060410, 0x9040410,
  0x51060010, 0x2041110, 0x53061510, 0x4041510, 0x55061110, 0x6041010, 0x57061410, 0x41410, 0x58061010, 0x1042110, 0x59062510, 0x2042510, 0x53062110, 0x4042010, 0x55062410, 0x6042410,
  0x57062010, 0x43110, 0x58063510, 0x1043510, 0x59063110, 0x2043010, 0x53063410, 0x20, 0x20, 0x20, 0x20, 0x20, 0x20, 0x20, 0x20, 0x20,
  0x4043410, 0x55063010, 0x40048018, 0x8050018, 0x41058018, 0x9060018, 0x62068018, 0x40000028, 0x3070018, 0x44078018, 0x5040218, 0x40048218, 0x8050218, 0x41058218, 0x9060218, 0x42068218,
  0x3070218, 0x44078218, 0x20000028, 0x10000028, 0x40, 0x40, 0x40, 0x40, 0x40, 0x40, 0x40, 0x40, 0x40, 0x40, 0x40, 0x40,
  0x40, 0x40, 0x40, 0x40, 0x40, 0x40, 0x40, 0x40, 0x40, 0x40, 0x40, 0x40, 0x40, 0x40, 0x40, 0x40,
  0x40, 0x40, 0x40, 0x40, 0x40, 0x40, 0x40, 0x40, 0x40, 0x40, 0x40, 0x40, 0x40, 0x40, 0x40, 0x40,
  0x80001, 0xc4000, 0x188000, 0x1cc000, 0x10000, 0x194000, 0x1d8000, 0x21c000, 0x260000, 0x24000, 0x44000, 0x100000, 0x148000, 0x18c000, 0x1d0000, 0x214000,
  0x18000, 0x25c000, 0x60000, 0xa4000, 0x100000, 0x140000, 0x400, 0x200000, 0x48000, 0x250000, 0x98000, 0xe0000, 0x100000, 0x9148000, 0x10000, 0x218000,
  0x60000, 0x240000, 0x88000, 0xd0000, 0x118000, 0x20000, 0x0, 0x0, 0x0, 0x0, 0x0, 0x0, 0x0, 0x0, 0x0, 0x0,
  0x0, 0x0, 0x0, 0x0, 0x0, 0x0, 0x0, 0x0, 0x0, 0x0, 0x0, 0x0, 0x0, 0x0, 0x0, 0x0,
  0x0, 0x0, 0x0, 0x0, 0x0, 0x0, 0x0, 0x0, 0x0, 0x0, 0x0, 0x0, 0x0, 0x0, 0x0, 0x0,
  0x80001, 0xc4000, 0x188000, 0x1cc000, 0x10000, 0x194000, 0x1d8000, 0x21c000, 0x260000, 0x24000, 0x44000, 0x100000, 0x148000, 0x18c000, 0x1d0000, 0x214000,
  0x18000, 0x25c000, 0x60000, 0xa4000, 0x100000, 0x140000, 0x400, 0x204000, 0x4c000, 0x254000, 0x9c000, 0xe4000, 0x104000, 0x914c000, 0x14000, 0x21c000,
  0x64000, 0x244000, 0x8c000, 0xd4000, 0x11c000, 0x24000, 0x4000, 0x0, 0x0, 0x0, 0x0, 0x0, 0x0, 0x0, 0x0, 0x0,
  0x0, 0x0, 0x0, 0x0, 0x0, 0x0, 0x0, 0x0, 0x0, 0x0, 0x0, 0x0, 0x0, 0x0, 0x0, 0x0,
  0x0, 0x0, 0x0, 0x0, 0x0, 0x0, 0x0, 0x0, 0x0, 0x0, 0x0, 0x0, 0x0, 0x0, 0x0, 0x0,
  0xf088001, 0xcc000, 0x190000, 0x1d4000, 0x18000, 0x19c000, 0x1e0000, 0x224000, 0x240000, 0x4000, 0x4c000, 0x108000, 0x150000, 0x194000, 0x1d8000, 0x21c000,
  0x20000, 0x264000, 0x40000, 0x84000, 0x100000, 0x140000, 0x0, 0xa210000, 0x9058000, 0x260000, 0xa4000, 0xc8000, 0x110000, 0x158000, 0x0, 0x204000,
  0x48000, 0x250000, 0x98000, 0xc0000, 0x104000, 0x8000, 0x10000, 0x0, 0x0, 0x0, 0x0, 0x0, 0x0, 0x0, 0x0, 0x0,
  0x0, 0x0, 0x0, 0x0, 0x0, 0x0, 0x0, 0x0, 0x0, 0x0, 0x0, 0x0, 0x0, 0x0, 0x0, 0x0,
  0x0, 0x0, 0x0, 0x0, 0x0, 0x0, 0x0, 0x0, 0x0, 0x0, 0x0, 0x0, 0x0, 0x0, 0x0, 0x0,
  0xf088001, 0xcc000, 0x190000, 0x1d4000, 0x18000, 0x19c000, 0x1e0000, 0x224000, 0x240000, 0x4000, 0x4c000, 0x108000, 0x150000, 0x194000, 0x1d8000, 0x21c000,
  0x20000, 0x264000, 0x40000, 0x84000, 0x100000, 0x140000, 0x0, 0xa214000, 0x905c000, 0x240000, 0x84000, 0xcc000, 0x114000, 0x15c000, 0x20000, 0x224000,
  0x4c000, 0x254000, 0x9c000, 0xe0000, 0x124000, 0xc000, 0x14000, 0x0, 0x0, 0x0, 0x0, 0x0, 0x0, 0x0, 0x0, 0x0,
  0x0, 0x0, 0x0, 0x0, 0x0, 0x0, 0x0, 0x0, 0x0, 0x0, 0x0, 0x0, 0x0, 0x0, 0x0, 0x0,
  0x0, 0x0, 0x0, 0x0, 0x0, 0x0, 0x0, 0x0, 0x0, 0x0, 0x0, 0x0, 0x0, 0x0, 0x0, 0x0,
  0x2040008, 0x3080008, 0x40c0008, 0x55000100, 0x6100008, 0x57140008, 0x8180008, 0x91c0008, 0x51000200, 0x200008, 0x52240008, 0x3280008, 0x442c0008, 0x5000300, 0x6300008, 0x57340008,
  0x8380008, 0x493c0008, 0x10, 0x41020410, 0x2000410, 0x53020010, 0x4000110, 0x55020510, 0x6000510, 0x57020110, 0x8001010, 0x59021410, 0x1410, 0x51021010, 0x2001110, 0x53021510,
  0x4001510, 0x55021110, 0x6002010, 0x57022410, 0x8002410, 0x59022010, 0x0, 0x51000208, 0x2040208, 0x53080208, 0x40c0208, 0x55000100, 0x6100208, 0x57140208, 0x8180208, 0x91c0208,
  0x51000200, 0x200208, 0x52240208, 0x3280208, 0x442c0208, 0x5000300, 0x6300208, 0x57340208, 0x8380208, 0x493c0208, 0x2110, 0x41022510, 0x2002510, 0x53022110, 0x20, 0x20,
  0x4003010, 0x55023410, 0x6003410, 0x57023010, 0x8003110, 0x59023510, 0x3510, 0x51023110, 0x4040110, 0x55060510, 0x6040510, 0x57060110, 0x8040010, 0x59060410, 0x18, 0x51000218,
  0x4008018, 0x55008218, 0x56010018, 0x7010218, 0x58018018, 0x9018218, 0x50020018, 0x1020218, 0x54028018, 0x5028218, 0x56030018, 0x2030218, 0x57038018, 0x8038218, 0x50040018, 0x9040410,
  0x51060010, 0x4041110, 0x55061510, 0x6041510, 0x52061110, 0x3041010, 0x57061410, 0x41410, 0x58061010, 0x1042110, 0x59062510, 0x4042510, 0x55062110, 0x2042010, 0x56062410, 0x3042410,
  0x57062010, 0x43110, 0x58063510, 0x1043510, 0x59063110, 0x4043010, 0x55063410, 0x20, 0x20, 0x20, 0x20, 0x20, 0x20, 0x20, 0x20, 0x20,
  0x2043410, 0x56063010, 0x40048018, 0x8050018, 0x41058018, 0x9060018, 0x64068018, 0x40000028, 0x5070018, 0x42078018, 0x6040218, 0x40048218, 0x8050218, 0x41058218, 0x9060218, 0x44068218,
  0x5070218, 0x42078218, 0x20000028, 0x10000028, 0x40, 0x40, 0x40, 0x40, 0x40, 0x40, 0x40, 0x40, 0x40, 0x40, 0x40, 0x40,
  0x40, 0x40, 0x40, 0x40, 0x40, 0x40, 0x40, 0x40, 0x40, 0x40, 0x40, 0x40, 0x40, 0x40, 0x40, 0x40,
  0x40, 0x40, 0x40, 0x40, 0x40, 0x40, 0x40, 0x40, 0x40, 0x40, 0x40, 0x40, 0x40, 0x40, 0x40, 0x40,
  0x80001, 0xc4000, 0xc8000, 0x1cc000, 0x10000, 0x14080, 0x58000, 0x11c000, 0x160000, 0x1a4000, 0x1c4000, 0x200000, 0x248000, 0xc000, 0x50000, 0x114000,
  0x158000, 0x19c000, 0xa0000, 0x1e4000, 0x200000, 0x0, 0x380, 0x200000, 0x48000, 0x250000, 0x118000, 0x160000, 0x80000, 0x188000, 0x10000, 0x218000,
  0x60000, 0x9240000, 0x108000, 0x150000, 0x98000, 0x20000, 0x0, 0x0, 0x0, 0x0, 0x0, 0x0, 0x0, 0x0, 0x0, 0x0,
  0x0, 0x0, 0x0, 0x0, 0x0, 0x0, 0x0, 0x0, 0x0, 0x0, 0x0, 0x0, 0x0, 0x0, 0x0, 0x0,
  0x0, 0x0, 0x0, 0x0, 0x0, 0x0, 0x0, 0x0, 0x0, 0x0, 0x0, 0x0, 0x0, 0x0, 0x0, 0x0,
  0x80001, 0xc4000, 0xc8000, 0x1cc000, 0x10000, 0x14080, 0x58000, 0x11c000, 0x160000, 0x1a4000, 0x1c4000, 0x200000, 0x248000, 0xc000, 0x50000, 0x114000,
  0x158000, 0x19c000, 0xa0000, 0x1e4000, 0x200000, 0x0, 0x380, 0x204000, 0x4c000, 0x254000, 0x11c000, 0x164000, 0x84000, 0x18c000, 0x14000, 0x21c000,
  0x64000, 0x9244000, 0x10c000, 0x154000, 0x9c000, 0x24000, 0x4000, 0x0, 0x0, 0x0, 0x0, 0x0, 0x0, 0x0, 0x0, 0x0,
  0x0, 0x0, 0x0, 0x0, 0x0, 0x0, 0x0, 0x0, 0x0, 0x0, 0x0, 0x0, 0x0, 0x0, 0x0, 0x0,
  0x0, 0x0, 0x0, 0x0, 0x0, 0x0, 0x0, 0x0, 0x0, 0x0, 0x0, 0x0, 0x0, 0x0, 0x0, 0x0,
  0x13080001, 0xc4000, 0xc8000, 0x1cc000, 0x10000, 0x14000, 0x58000, 0x11c000, 0x160000, 0x1a4000, 0x1c4000, 0x200000, 0x248000, 0xc000, 0x50000, 0x114000,
  0x158000, 0x19c000, 0xa0000, 0x1e4000, 0x200000, 0x0, 0x0, 0x6210000, 0x58000, 0x260000, 0x9124000, 0x150000, 0x98000, 0x18c000, 0x0, 0x204000,
  0x50000, 0x248000, 0x10c000, 0x140000, 0x84000, 0x10000, 0x8000, 0x0, 0x0, 0x0, 0x0, 0x0, 0x0, 0x0, 0x0, 0x0,
  0x0, 0x0, 0x0, 0x0, 0x0, 0x0, 0x0, 0x0, 0x0, 0x0, 0x0, 0x0, 0x0, 0x0, 0x0, 0x0,
  0x0, 0x0, 0x0, 0x0, 0x0, 0x0, 0x0, 0x0, 0x0, 0x0, 0x0, 0x0, 0x0, 0x0, 0x0, 0x0,
  0x13080001, 0xc4000, 0xc8000, 0x1cc000, 0x10000, 0x14000, 0x58000, 0x11c000, 0x160000, 0x1a4000, 0x1c4000, 0x200000, 0x248000, 0xc000, 0x50000, 0x114000,
  0x158000, 0x19c000, 0xa0000, 0x1e4000, 0x200000, 0x0, 0x0, 0x6214000, 0x5c000, 0x264000, 0x9104000, 0x154000, 0x88000, 0x19c000, 0x20000, 0x224000,
  0x54000, 0x258000, 0x11c000, 0x160000, 0xa4000, 0x14000, 0x18000, 0x0, 0x0, 0x0, 0x0, 0x0, 0x0, 0x0, 0x0, 0x0,
  0x0, 0x0, 0x0, 0x0, 0x0, 0x0, 0x0, 0x0, 0x0, 0x0, 0x0, 0x0, 0x0, 0x0, 0x0, 0x0,
  0x0, 0x0, 0x0, 0x0, 0x0, 0x0, 0x0, 0x0, 0x0, 0x0, 0x0, 0x0, 0x0, 0x0, 0x0, 0x0,
  0x2040008, 0x3080008, 0x40c0008, 0x55000100, 0x6100008, 0x57140008, 0x8180008, 0x91c0008, 0x51000200, 0x200008, 0x52240008, 0x3280008, 0x442c0008, 0x5000300, 0x6300008, 0x57340008,
  0x8380008, 0x493c0008, 0x10, 0x41020410, 0x2000410, 0x53020010, 0x4000110, 0x55020510, 0x6000510, 0x57020110, 0x8001010, 0x59021410, 0x1410, 0x51021010, 0x2001110, 0x53021510,
  0x4001510, 0x55021110, 0x6002010, 0x57022410, 0x8002410, 0x59022010, 0x2110, 0x51022510, 0x2002510, 0x53022110, 0x20, 0x20, 0x20, 0x20, 0x20, 0x20,
  0x20, 0x20, 0x20, 0x20, 0x20, 0x20, 0x20, 0x20, 0x20, 0x20, 0x20, 0x20, 0x20, 0x20, 0x20, 0x20,
  0x4003010, 0x55023410, 0x6000000, 0x57000208, 0x8040208, 0x59080208, 0xc0208, 0x51000100, 0x2100208, 0x53140208, 0x4180208, 0x51c0208, 0x57000200, 0x6200208, 0x58240208, 0x280208,
  0x492c0208, 0x1000300, 0x2300208, 0x53340208, 0x4380208, 0x453c0208, 0x6003410, 0x47023010, 0x8003110, 0x50023510, 0x9003510, 0x51023110, 0x4040110, 0x55060510, 0x6040510, 0x57060110,
  0x40010, 0x58060410, 0x1040410, 0x59060010, 0x4041110, 0x55061510, 0x6041510, 0x57061110, 0x18, 0x58000218, 0x1008018, 0x59008218, 0x54010018, 0x5010218, 0x56018018, 0x7018218,
  0x50020018, 0x1020218, 0x58028018, 0x4028218, 0x59030018, 0x2030218, 0x55038018, 0x6038218, 0x50040018, 0x7041010, 0x51061410, 0x8041410, 0x54061010, 0x9042110, 0x52062510, 0x20,
  0x3042510, 0x55062110, 0x42010, 0x56062410, 0x1042410, 0x57062010, 0x4043110, 0x58063510, 0x2043510, 0x59063110, 0x3043010, 0x55063410, 0x43410, 0x56063010, 0x44048018, 0x8050018,
  0x42058018, 0x9060018, 0x63068018, 0x40000028, 0x5070018, 0x40078018, 0x6040218, 0x44048218, 0x8050218, 0x42058218, 0x9060218, 0x43068218, 0x5070218, 0x40078218, 0x20000028, 0x10000028,
  0x40, 0x40, 0x40, 0x40, 0x40, 0x40, 0x40, 0x40, 0x40, 0x40, 0x40, 0x40, 0x40, 0x40, 0x40, 0x40,
  0x40, 0x40, 0x40, 0x40, 0x40, 0x40, 0x40, 0x40, 0x40, 0x40, 0x40, 0x40, 0x40, 0x40, 0x40, 0x40,
  0x80001, 0xc4000, 0x48000, 0x1cc000, 0x10000, 0x14140, 0x218000, 0x5c000, 0x260000, 0x124000, 0x144000, 0x180000, 0x1c8000, 0xc000, 0x50000, 0x214000,
  0x118000, 0x25c000, 0xa0000, 0x164000, 0x180000, 0x0, 0x1002c0, 0x200000, 0x88000, 0x250000, 0xd8000, 0x160000, 0x0, 0x188000, 0x110000, 0x218000,
  0xa0000, 0x240000, 0xc8000, 0x150000, 0x9018000, 0x20000, 0x24000, 0x0, 0x0, 0x0, 0x0, 0x0, 0x0, 0x0, 0x0, 0x0,
  0x0, 0x0, 0x0, 0x0, 0x0, 0x0, 0x0, 0x0, 0x0, 0x0, 0x0, 0x0, 0x0, 0x0, 0x0, 0x0,
  0x0, 0x0, 0x0, 0x0, 0x0, 0x0, 0x0, 0x0, 0x0, 0x0, 0x0, 0x0, 0x0, 0x0, 0x0, 0x0,
  0x80001, 0xc4000, 0x48000, 0x1cc000, 0x10000, 0x14140, 0x218000, 0x5c000, 0x260000, 0x124000, 0x144000, 0x180000, 0x1c8000, 0xc000, 0x50000, 0x214000,
  0x118000, 0x25c000, 0xa0000, 0x164000, 0x180000, 0x0, 0x1002c0, 0x204000, 0x8c000, 0x254000, 0xdc000, 0x164000, 0x4000, 0x18c000, 0x114000, 0x21c000,
  0xa4000, 0x244000, 0xcc000, 0x154000, 0x901c000, 0x0, 0x4000, 0x0, 0x0, 0x0, 0x0, 0x0, 0x0, 0x0, 0x0, 0x0,
  0x0, 0x0, 0x0, 0x0, 0x0, 0x0, 0x0, 0x0, 0x0, 0x0, 0x0, 0x0, 0x0, 0x0, 0x0, 0x0,
  0x0, 0x0, 0x0, 0x0, 0x0, 0x0, 0x0, 0x0, 0x0, 0x0, 0x0, 0x0, 0x0, 0x0, 0x0, 0x0,
  0x16098001, 0xdc000, 0x60000, 0x1e4000, 0x0, 0x4000, 0x208000, 0x4c000, 0x250000, 0x114000, 0x15c000, 0x198000, 0x1e0000, 0x0, 0x64000, 0x204000,
  0x108000, 0x24c000, 0x90000, 0x154000, 0x180000, 0x0, 0x100000, 0x3210000, 0x98000, 0x240000, 0xc4000, 0x150000, 0x18000, 0x919c000, 0x120000, 0x224000,
  0x8c000, 0x240000, 0xc4000, 0x150000, 0x8000, 0xc000, 0x0, 0x0, 0x0, 0x0, 0x0, 0x0, 0x0, 0x0, 0x0, 0x0,
  0x0, 0x0, 0x0, 0x0, 0x0, 0x0, 0x0, 0x0, 0x0, 0x0, 0x0, 0x0, 0x0, 0x0, 0x0, 0x0,
  0x0, 0x0, 0x0, 0x0, 0x0, 0x0, 0x0, 0x0, 0x0, 0x0, 0x0, 0x0, 0x0, 0x0, 0x0, 0x0,
  0x16098001, 0xdc000, 0x60000, 0x1e4000, 0x0, 0x4000, 0x208000, 0x4c000, 0x250000, 0x114000, 0x15c000, 0x198000, 0x1e0000, 0x0, 0x64000, 0x204000,
  0x108000, 0x24c000, 0x90000, 0x154000, 0x180000, 0x0, 0x100000, 0x3214000, 0x9c000, 0x260000, 0xe4000, 0x154000, 0x1c000, 0x9184000, 0x110000, 0x208000,
  0x94000, 0x258000, 0xdc000, 0x160000, 0x24000, 0x14000, 0x18000, 0x0, 0x0, 0x0, 0x0, 0x0, 0x0, 0x0, 0x0, 0x0,
  0x0, 0x0, 0x0, 0x0, 0x0, 0x0, 0x0, 0x0, 0x0, 0x0, 0x0, 0x0, 0x0, 0x0, 0x0, 0x0,
  0x0, 0x0, 0x0, 0x0, 0x0, 0x0, 0x0, 0x0, 0x0, 0x0, 0x0, 0x0, 0x0, 0x0, 0x0, 0x0,
  0x2040008, 0x3080008, 0x40c0008, 0x55000100, 0x6100008, 0x57140008, 0x8180008, 0x91c0008, 0x51000200, 0x200008, 0x52240008, 0x3280008, 0x442c0008, 0x5000300, 0x6300008, 0x57340008,
  0x8380008, 0x493c0008, 0x10, 0x41020410, 0x2000410, 0x53020010, 0x4000110, 0x55020510, 0x6000510, 0x57020110, 0x8001010, 0x59021410, 0x1410, 0x51021010, 0x2001110, 0x53021510,
  0x4001510, 0x55021110, 0x6002010, 0x57022410, 0x8002410, 0x59022010, 0x2110, 0x51022510, 0x2002510, 0x53022110, 0x4003010, 0x55023410, 0x6003410, 0x57023010, 0x8003110, 0x59023510,
  0x20, 0x20, 0x20, 0x20, 0x20, 0x20, 0x20, 0x20, 0x20, 0x20, 0x20, 0x20, 0x20, 0x20, 0x20, 0x20,
  0x3510, 0x51023110, 0x2000000, 0x53000208, 0x4040208, 0x55080208, 0x60c0208, 0x57000100, 0x8100208, 0x59140208, 0x180208, 0x11c0208, 0x53000200, 0x2200208, 0x54240208, 0x5280208,
  0x462c0208, 0x7000300, 0x8300208, 0x59340208, 0x380208, 0x413c0208, 0x34040110, 0x5060510, 0x46040510, 0x7060110, 0x8040010, 0x59060410, 0x40410, 0x51060010, 0x4041110, 0x55061510,
  0x6041510, 0x57061110, 0x8041010, 0x59061410, 0x41410, 0x51061010, 0x4042110, 0x55062510, 0x6000018, 0x57000218, 0x8008018, 0x59008218, 0x50010018, 0x1010218, 0x54018018, 0x5018218,
  0x56020018, 0x7020218, 0x58028018, 0x28218, 0x59030018, 0x1030218, 0x52038018, 0x4038218, 0x55040018, 0x6042510, 0x57062110, 0x8042010, 0x50062410, 0x9042410, 0x51062010, 0x20,
  0x2043110, 0x53063510, 0x4043510, 0x55063110, 0x6043010, 0x57063410, 0x43410, 0x58063010, 0x42048018, 0x3050018, 0x44058018, 0x5060018, 0x66068018, 0x40000028, 0x7070018, 0x40078018,
  0x8040218, 0x42048218, 0x3050218, 0x44058218, 0x5060218, 0x46068218, 0x7070218, 0x40078218, 0x20000028, 0x10000028, 0x40, 0x40, 0x40, 0x40, 0x40, 0x40,
  0x40, 0x40, 0x40, 0x40, 0x40, 0x40, 0x40, 0x40, 0x40, 0x40, 0x40, 0x40, 0x40, 0x40, 0x40, 0x40,
  0x40, 0x40, 0x40, 0x40, 0x40, 0x40, 0x40, 0x40, 0x40, 0x40, 0x40, 0x40, 0x40, 0x40, 0x40, 0x40,
  0x80001, 0xc4000, 0x48000, 0x24c000, 0x10000, 0x194200, 0x1d8000, 0x21c000, 0x260000, 0x24000, 0x44000, 0x100000, 0x148000, 0x18c000, 0x1d0000, 0x214000,
  0x18000, 0x25c000, 0x60000, 0xa4000, 0x100000, 0x140000, 0x80200, 0xc0000, 0x108000, 0x150000, 0x198000, 0x1e0000, 0x0, 0x208000, 0x90000, 0xd8000,
  0x120000, 0x140000, 0x188000, 0x1d0000, 0x18000, 0x20000, 0x0, 0x9000000, 0x0, 0x0, 0x0, 0x0, 0x0, 0x0, 0x0, 0x0,
  0x0, 0x0, 0x0, 0x0, 0x0, 0x0, 0x0, 0x0, 0x0, 0x0, 0x0, 0x0, 0x0, 0x0, 0x0, 0x0,
  0x0, 0x0, 0x0, 0x0, 0x0, 0x0, 0x0, 0x0, 0x0, 0x0, 0x0, 0x0, 0x0, 0x0, 0x0, 0x0,
  0x80001, 0xc4000, 0x48000, 0x24c000, 0x10000, 0x194200, 0x1d8000, 0x21c000, 0x260000, 0x24000, 0x44000, 0x100000, 0x148000, 0x18c000, 0x1d0000, 0x214000,
  0x18000, 0x25c000, 0x60000, 0xa4000, 0x100000, 0x140000, 0x80200, 0xc4000, 0x10c000, 0x154000, 0x19c000, 0x1e4000, 0x4000, 0x20c000, 0x94000, 0xdc000,
  0x124000, 0x144000, 0x18c000, 0x1d4000, 0x1c000, 0x24000, 0x4000, 0x9000000, 0x0, 0x0, 0x0, 0x0, 0x0, 0x0, 0x0, 0x0,
  0x0, 0x0, 0x0, 0x0, 0x0, 0x0, 0x0, 0x0, 0x0, 0x0, 0x0, 0x0, 0x0, 0x0, 0x0, 0x0,
  0x0, 0x0, 0x0, 0x0, 0x0, 0x0, 0x0, 0x0, 0x0, 0x0, 0x0, 0x0, 0x0, 0x0, 0x0, 0x0,
  0x19088001, 0xcc000, 0x50000, 0x254000, 0x18000, 0x19c000, 0x1e0000, 0x224000, 0x240000, 0x4000, 0x4c000, 0x108000, 0x150000, 0x194000, 0x1d8000, 0x21c000,
  0x20000, 0x264000, 0x40000, 0x84000, 0x100000, 0x140000, 0x80000, 0xd0000, 0x118000, 0x160000, 0x180000, 0x1d0000, 0x18000, 0x220000, 0x80000, 0xd0000,
  0x9118000, 0x160000, 0x1a4000, 0x1c8000, 0x10000, 0x18000, 0x0, 0x0, 0x0, 0x0, 0x0, 0x0, 0x0, 0x0, 0x0, 0x0,
  0x0, 0x0, 0x0, 0x0, 0x0, 0x0, 0x0, 0x0, 0x0, 0x0, 0x0, 0x0, 0x0, 0x0, 0x0, 0x0,
  0x0, 0x0, 0x0, 0x0, 0x0, 0x0, 0x0, 0x0, 0x0, 0x0, 0x0, 0x0, 0x0, 0x0, 0x0, 0x0,
  0x19088001, 0xcc000, 0x50000, 0x254000, 0x18000, 0x19c000, 0x1e0000, 0x224000, 0x240000, 0x4000, 0x4c000, 0x108000, 0x150000, 0x194000, 0x1d8000, 0x21c000,
  0x20000, 0x264000, 0x40000, 0x84000, 0x100000, 0x140000, 0x80000, 0xd4000, 0x11c000, 0x164000, 0x184000, 0x1d4000, 0x1c000, 0x224000, 0x84000, 0xd4000,
  0x911c000, 0x140000, 0x184000, 0x1cc000, 0x14000, 0x1c000, 0x20000, 0x0, 0x0, 0x0, 0x0, 0x0, 0x0, 0x0, 0x0, 0x0,
  0x0, 0x0, 0x0, 0x0, 0x0, 0x0, 0x0, 0x0, 0x0, 0x0, 0x0, 0x0, 0x0, 0x0, 0x0, 0x0,
  0x0, 0x0, 0x0, 0x0, 0x0, 0x0, 0x0, 0x0, 0x0, 0x0, 0x0, 0x0, 0x0, 0x0, 0x0, 0x0,
};

DI unsigned rfl(unsigned x) { return __builtin_amdgcn_readfirstlane(x); }
DI unsigned cvtpk(float lo, float hi) { f32x2 v = {lo, hi}; bf16x2 b = __builtin_convertvector(v, bf16x2); return __builtin_bit_cast(unsigned, b); }
#define RP 0x36781
#define TP 0x5402
DI unsigned dep(unsigned v, unsigned pos, int n) { unsigned r = 0; for (int i = 0; i < n; ++i) r |= (v >> i & 1) << (pos >> 4 * i & 15); return r; }
DI unsigned lin(unsigned v, unsigned m) { unsigned r = 0; for (int i = 0; i < 7; ++i) r ^= -(v >> i & 1) & (m >> 4 * i); return r; }
DI int lane_id() { int l; asm volatile("v_mbcnt_lo_u32_b32 %0, -1, 0\n\tv_mbcnt_hi_u32_b32 %0, -1, %0" : "=v"(l)); return l; }

#define COMPUTE_ASM \
  "v_mov_b64 v[0:1], 0\n" \
  "v_mov_b64 v[2:3], 0\n" \
  "v_mov_b64 v[4:5], 0\n" \
  "v_mov_b64 v[6:7], 0\n" \
  "v_mov_b64 v[8:9], 0\n" \
  "v_mov_b64 v[10:11], 0\n" \
  "v_mov_b64 v[12:13], 0\n" \
  "v_mov_b64 v[14:15], 0\n" \
  "v_mov_b64 v[16:17], 0\n" \
  "v_mov_b64 v[18:19], 0\n" \
  "v_mov_b64 v[20:21], 0\n" \
  "v_mov_b64 v[22:23], 0\n" \
  "v_mov_b64 v[24:25], 0\n" \
  "v_mov_b64 v[26:27], 0\n" \
  "v_mov_b64 v[28:29], 0\n" \
  "v_mov_b64 v[30:31], 0\n" \
  "v_mov_b64 v[32:33], 0\n" \
  "v_mov_b64 v[34:35], 0\n" \
  "v_mov_b64 v[36:37], 0\n" \
  "v_mov_b64 v[38:39], 0\n" \
  "v_mov_b64 v[40:41], 0\n" \
  "v_mov_b64 v[42:43], 0\n" \
  "v_mov_b64 v[44:45], 0\n" \
  "v_mov_b64 v[46:47], 0\n" \
  "v_mov_b64 v[48:49], 0\n" \
  "v_mov_b64 v[50:51], 0\n" \
  "v_mov_b64 v[52:53], 0\n" \
  "v_mov_b64 v[54:55], 0\n" \
  "v_mov_b64 v[56:57], 0\n" \
  "v_mov_b64 v[58:59], 0\n" \
  "v_mov_b64 v[60:61], 0\n" \
  "v_mov_b64 v[62:63], 0\n" \
  "s_waitcnt vmcnt(0)\n" \
  "s_cmp_lg_u32 s45, 3\n" \
  "s_cbranch_scc1 .Lnos_%=\n" \
  "v_mbcnt_lo_u32_b32 v166, -1, 0\n" \
  "v_mbcnt_hi_u32_b32 v166, -1, v166\n" \
  "v_lshlrev_b32 v166, 4, v166\n" \
  "v_bfe_u32 v166, v166, 2, 7\n" \
  "global_load_dword v109, v166, s[36:37] sc1\n" \
  ".Lnos_%=:\n" \
  "v_readlane_b32 s58, v160, 0\n" \
  "v_readlane_b32 s55, v160, 1\n" \
  "v_readlane_b32 s50, v160, 2\n" \
  "s_and_b32 s59, s58, 0x3c000\n" \
  "s_add_u32 s59, s59, s41\n" \
  "v_add_u32 v150, s59, v161\n" \
  "s_and_b32 s52, s55, 0x3c000\n" \
  "v_add_u32 v146, s52, v144\n" \
  "v_xor_b32 v147, 32, v146\n" \
  "s_and_b32 s52, s50, 0x3c000\n" \
  "v_add_u32 v148, s52, v144\n" \
  "v_xor_b32 v149, 32, v148\n" \
  "s_mov_b32 s54, 3\n" \
  "s_and_b64 vcc, s[58:59], 1\n" \
  "s_lshr_b32 s51, s58, 24\n" \
  "s_cbranch_scc1 .Lstub_p0_%=\n" \
  ".Lcont_p0_%=:\n" \
  "s_barrier\n" \
  "ds_read_b128 v[64:67], v150\n" \
  "ds_read_b128 v[68:71], v150 offset:4096\n" \
  "ds_read_b128 v[72:75], v150 offset:512\n" \
  "ds_read_b128 v[76:79], v150 offset:4608\n" \
  "ds_read_b64_tr_b16 v[112:113], v146\n" \
  "ds_read_b64_tr_b16 v[114:115], v146 offset:256\n" \
  "ds_read_b64_tr_b16 v[116:117], v147\n" \
  "ds_read_b64_tr_b16 v[118:119], v147 offset:256\n" \
  "ds_read_b64_tr_b16 v[120:121], v146 offset:512\n" \
  "ds_read_b64_tr_b16 v[122:123], v146 offset:768\n" \
  "ds_read_b64_tr_b16 v[124:125], v147 offset:512\n" \
  "ds_read_b64_tr_b16 v[126:127], v147 offset:768\n" \
  "ds_read_b64_tr_b16 v[128:129], v146 offset:1024\n" \
  "ds_read_b64_tr_b16 v[130:131], v146 offset:1280\n" \
  "ds_read_b64_tr_b16 v[132:133], v147 offset:1024\n" \
  "ds_read_b64_tr_b16 v[134:135], v147 offset:1280\n" \
  "ds_read_b64_tr_b16 v[136:137], v146 offset:1536\n" \
  "ds_read_b64_tr_b16 v[138:139], v146 offset:1792\n" \
  "ds_read_b64_tr_b16 v[140:141], v147 offset:1536\n" \
  "ds_read_b64_tr_b16 v[142:143], v147 offset:1792\n" \
  "s_waitcnt lgkmcnt(0)\n" \
  ".Lqloop_%=:\n" \
  "v_mfma_f32_16x16x32_bf16 v[0:3], v[112:115], v[64:67], v[0:3]\n" \
  "s_waitcnt lgkmcnt(13)\n" \
  "s_barrier\n" \
  "ds_read_b128 v[80:83], v150 offset:1024\n" \
  "v_mfma_f32_16x16x32_bf16 v[8:11], v[112:115], v[68:71], v[8:11]\n" \
  "ds_read_b64_tr_b16 v[112:113], v148\n" \
  "v_mfma_f32_16x16x32_bf16 v[4:7], v[116:119], v[64:67], v[4:7]\n" \
  "ds_read_b64_tr_b16 v[114:115], v148 offset:256\n" \
  "v_readlane_b32 s50, v160, s54\n" \
  "v_mfma_f32_16x16x32_bf16 v[12:15], v[116:119], v[68:71], v[12:15]\n" \
  "s_waitcnt lgkmcnt(13)\n" \
  "ds_read_b64_tr_b16 v[116:117], v149\n" \
  "v_mfma_f32_16x16x32_bf16 v[16:19], v[120:123], v[64:67], v[16:19]\n" \
  "s_add_u32 s54, s54, 1\n" \
  "ds_read_b64_tr_b16 v[118:119], v149 offset:256\n" \
  "v_mfma_f32_16x16x32_bf16 v[24:27], v[120:123], v[68:71], v[24:27]\n" \
  "ds_read_b64_tr_b16 v[120:121], v148 offset:512\n" \
  "ds_read_b128 v[84:87], v150 offset:5120\n" \
  "v_mfma_f32_16x16x32_bf16 v[20:23], v[124:127], v[64:67], v[20:23]\n" \
  "s_and_b32 s52, s50, 0x3c000\n" \
  "ds_read_b64_tr_b16 v[122:123], v148 offset:768\n" \
  "v_mfma_f32_16x16x32_bf16 v[28:31], v[124:127], v[68:71], v[28:31]\n" \
  "s_waitcnt lgkmcnt(13)\n" \
  "ds_read_b64_tr_b16 v[124:125], v149 offset:512\n" \
  "v_mfma_f32_16x16x32_bf16 v[32:35], v[128:131], v[64:67], v[32:35]\n" \
  "ds_read_b64_tr_b16 v[126:127], v149 offset:768\n" \
  "v_add_u32 v146, s52, v144\n" \
  "v_mfma_f32_16x16x32_bf16 v[40:43], v[128:131], v[68:71], v[40:43]\n" \
  "ds_read_b64_tr_b16 v[128:129], v148 offset:1024\n" \
  "ds_read_b128 v[88:91], v150 offset:1536\n" \
  "v_mfma_f32_16x16x32_bf16 v[36:39], v[132:135], v[64:67], v[36:39]\n" \
  "ds_read_b64_tr_b16 v[130:131], v148 offset:1280\n" \
  "v_xor_b32 v147, 32, v146\n" \
  "v_mfma_f32_16x16x32_bf16 v[44:47], v[132:135], v[68:71], v[44:47]\n" \
  "s_waitcnt lgkmcnt(13)\n" \
  "ds_read_b64_tr_b16 v[132:133], v149 offset:1024\n" \
  "v_mfma_f32_16x16x32_bf16 v[48:51], v[136:139], v[64:67], v[48:51]\n" \
  "ds_read_b64_tr_b16 v[134:135], v149 offset:1280\n" \
  "v_mfma_f32_16x16x32_bf16 v[56:59], v[136:139], v[68:71], v[56:59]\n" \
  "ds_read_b64_tr_b16 v[136:137], v148 offset:1536\n" \
  "ds_read_b128 v[92:95], v150 offset:5632\n" \
  "v_mfma_f32_16x16x32_bf16 v[52:55], v[140:143], v[64:67], v[52:55]\n" \
  "ds_read_b64_tr_b16 v[138:139], v148 offset:1792\n" \
  "v_mfma_f32_16x16x32_bf16 v[60:63], v[140:143], v[68:71], v[60:63]\n" \
  "s_waitcnt lgkmcnt(12)\n" \
  "ds_read_b64_tr_b16 v[140:141], v149 offset:1536\n" \
  "ds_read_b64_tr_b16 v[142:143], v149 offset:1792\n" \
  "v_mfma_f32_16x16x32_bf16 v[0:3], v[112:115], v[72:75], v[0:3]\n" \
  "s_cbranch_vccz .Lob1_%=\n" \
  ".Lnb1_%=:\n" \
  "v_mfma_f32_16x16x32_bf16 v[8:11], v[112:115], v[76:79], v[8:11]\n" \
  "ds_read_b64_tr_b16 v[112:113], v146\n" \
  "v_mfma_f32_16x16x32_bf16 v[4:7], v[116:119], v[72:75], v[4:7]\n" \
  "ds_read_b64_tr_b16 v[114:115], v146 offset:256\n" \
  "v_readlane_b32 s50, v160, s54\n" \
  "v_mfma_f32_16x16x32_bf16 v[12:15], v[116:119], v[76:79], v[12:15]\n" \
  "s_waitcnt lgkmcnt(13)\n" \
  "ds_read_b64_tr_b16 v[116:117], v147\n" \
  "v_mfma_f32_16x16x32_bf16 v[16:19], v[120:123], v[72:75], v[16:19]\n" \
  "s_add_u32 s54, s54, 1\n" \
  "ds_read_b64_tr_b16 v[118:119], v147 offset:256\n" \
  "v_mfma_f32_16x16x32_bf16 v[24:27], v[120:123], v[76:79], v[24:27]\n" \
  "s_waitcnt lgkmcnt(13)\n" \
  "ds_read_b64_tr_b16 v[120:121], v146 offset:512\n" \
  "v_mfma_f32_16x16x32_bf16 v[20:23], v[124:127], v[72:75], v[20:23]\n" \
  "s_and_b32 s52, s50, 0x3c000\n" \
  "ds_read_b64_tr_b16 v[122:123], v146 offset:768\n" \
  "v_mfma_f32_16x16x32_bf16 v[28:31], v[124:127], v[76:79], v[28:31]\n" \
  "s_waitcnt lgkmcnt(13)\n" \
  "ds_read_b64_tr_b16 v[124:125], v147 offset:512\n" \
  "v_mfma_f32_16x16x32_bf16 v[32:35], v[128:131], v[72:75], v[32:35]\n" \
  "ds_read_b64_tr_b16 v[126:127], v147 offset:768\n" \
  "v_add_u32 v148, s52, v144\n" \
  "v_mfma_f32_16x16x32_bf16 v[40:43], v[128:131], v[76:79], v[40:43]\n" \
  "s_waitcnt lgkmcnt(13)\n" \
  "ds_read_b64_tr_b16 v[128:129], v146 offset:1024\n" \
  "v_mfma_f32_16x16x32_bf16 v[36:39], v[132:135], v[72:75], v[36:39]\n" \
  "ds_read_b64_tr_b16 v[130:131], v146 offset:1280\n" \
  "v_xor_b32 v149, 32, v148\n" \
  "v_mfma_f32_16x16x32_bf16 v[44:47], v[132:135], v[76:79], v[44:47]\n" \
  "s_waitcnt lgkmcnt(12)\n" \
  "ds_read_b64_tr_b16 v[132:133], v147 offset:1024\n" \
  "v_mfma_f32_16x16x32_bf16 v[48:51], v[136:139], v[72:75], v[48:51]\n" \
  "ds_read_b64_tr_b16 v[134:135], v147 offset:1280\n" \
  "v_readlane_b32 s58, v160, s54\n" \
  "v_mfma_f32_16x16x32_bf16 v[56:59], v[136:139], v[76:79], v[56:59]\n" \
  "s_waitcnt lgkmcnt(12)\n" \
  "ds_read_b64_tr_b16 v[136:137], v146 offset:1536\n" \
  "v_mfma_f32_16x16x32_bf16 v[52:55], v[140:143], v[72:75], v[52:55]\n" \
  "s_add_u32 s54, s54, 1\n" \
  "ds_read_b64_tr_b16 v[138:139], v146 offset:1792\n" \
  "v_mfma_f32_16x16x32_bf16 v[60:63], v[140:143], v[76:79], v[60:63]\n" \
  "s_waitcnt lgkmcnt(12)\n" \
  "ds_read_b64_tr_b16 v[140:141], v147 offset:1536\n" \
  "ds_read_b64_tr_b16 v[142:143], v147 offset:1792\n" \
  "v_mfma_f32_16x16x32_bf16 v[0:3], v[112:115], v[80:83], v[0:3]\n" \
  "s_barrier\n" \
  "v_mfma_f32_16x16x32_bf16 v[8:11], v[112:115], v[84:87], v[8:11]\n" \
  "s_waitcnt lgkmcnt(12)\n" \
  "ds_read_b64_tr_b16 v[112:113], v148\n" \
  "v_mfma_f32_16x16x32_bf16 v[4:7], v[116:119], v[80:83], v[4:7]\n" \
  "s_and_b32 s59, s58, 0x3c000\n" \
  "ds_read_b64_tr_b16 v[114:115], v148 offset:256\n" \
  "v_readlane_b32 s50, v160, s54\n" \
  "v_mfma_f32_16x16x32_bf16 v[12:15], v[116:119], v[84:87], v[12:15]\n" \
  "s_waitcnt lgkmcnt(12)\n" \
  "ds_read_b64_tr_b16 v[116:117], v149\n" \
  "v_mfma_f32_16x16x32_bf16 v[16:19], v[120:123], v[80:83], v[16:19]\n" \
  "s_add_u32 s54, s54, 1\n" \
  "ds_read_b64_tr_b16 v[118:119], v149 offset:256\n" \
  "v_mfma_f32_16x16x32_bf16 v[24:27], v[120:123], v[84:87], v[24:27]\n" \
  "s_waitcnt lgkmcnt(12)\n" \
  "ds_read_b64_tr_b16 v[120:121], v148 offset:512\n" \
  "v_mfma_f32_16x16x32_bf16 v[20:23], v[124:127], v[80:83], v[20:23]\n" \
  "s_and_b32 s52, s50, 0x3c000\n" \
  "ds_read_b64_tr_b16 v[122:123], v148 offset:768\n" \
  "v_mfma_f32_16x16x32_bf16 v[28:31], v[124:127], v[84:87], v[28:31]\n" \
  "s_waitcnt lgkmcnt(12)\n" \
  "ds_read_b64_tr_b16 v[124:125], v149 offset:512\n" \
  "v_mfma_f32_16x16x32_bf16 v[32:35], v[128:131], v[80:83], v[32:35]\n" \
  "ds_read_b64_tr_b16 v[126:127], v149 offset:768\n" \
  "v_add_u32 v146, s52, v144\n" \
  "v_mfma_f32_16x16x32_bf16 v[40:43], v[128:131], v[84:87], v[40:43]\n" \
  "s_waitcnt lgkmcnt(12)\n" \
  "ds_read_b64_tr_b16 v[128:129], v148 offset:1024\n" \
  "v_mfma_f32_16x16x32_bf16 v[36:39], v[132:135], v[80:83], v[36:39]\n" \
  "ds_read_b64_tr_b16 v[130:131], v148 offset:1280\n" \
  "v_xor_b32 v147, 32, v146\n" \
  "v_mfma_f32_16x16x32_bf16 v[44:47], v[132:135], v[84:87], v[44:47]\n" \
  "s_waitcnt lgkmcnt(12)\n" \
  "ds_read_b64_tr_b16 v[132:133], v149 offset:1024\n" \
  "v_mfma_f32_16x16x32_bf16 v[48:51], v[136:139], v[80:83], v[48:51]\n" \
  "s_add_u32 s59, s59, s41\n" \
  "ds_read_b64_tr_b16 v[134:135], v149 offset:1280\n" \
  "v_mfma_f32_16x16x32_bf16 v[56:59], v[136:139], v[84:87], v[56:59]\n" \
  "s_waitcnt lgkmcnt(12)\n" \
  "ds_read_b64_tr_b16 v[136:137], v148 offset:1536\n" \
  "v_mfma_f32_16x16x32_bf16 v[52:55], v[140:143], v[80:83], v[52:55]\n" \
  "ds_read_b64_tr_b16 v[138:139], v148 offset:1792\n" \
  "v_add_u32 v150, s59, v161\n" \
  "v_mfma_f32_16x16x32_bf16 v[60:63], v[140:143], v[84:87], v[60:63]\n" \
  "s_waitcnt lgkmcnt(12)\n" \
  "ds_read_b64_tr_b16 v[140:141], v149 offset:1536\n" \
  "ds_read_b64_tr_b16 v[142:143], v149 offset:1792\n" \
  "v_mfma_f32_16x16x32_bf16 v[0:3], v[112:115], v[88:91], v[0:3]\n" \
  "s_cbranch_vccz .Lob3_%=\n" \
  ".Lnb3_%=:\n" \
  "ds_read_b128 v[64:67], v150\n" \
  "v_mfma_f32_16x16x32_bf16 v[8:11], v[112:115], v[92:95], v[8:11]\n" \
  "s_waitcnt lgkmcnt(13)\n" \
  "ds_read_b64_tr_b16 v[112:113], v146\n" \
  "v_mfma_f32_16x16x32_bf16 v[4:7], v[116:119], v[88:91], v[4:7]\n" \
  "ds_read_b64_tr_b16 v[114:115], v146 offset:256\n" \
  "v_readlane_b32 s50, v160, s54\n" \
  "v_mfma_f32_16x16x32_bf16 v[12:15], v[116:119], v[92:95], v[12:15]\n" \
  "s_waitcnt lgkmcnt(13)\n" \
  "ds_read_b64_tr_b16 v[116:117], v147\n" \
  "v_mfma_f32_16x16x32_bf16 v[16:19], v[120:123], v[88:91], v[16:19]\n" \
  "s_add_u32 s54, s54, 1\n" \
  "ds_read_b64_tr_b16 v[118:119], v147 offset:256\n" \
  "v_mfma_f32_16x16x32_bf16 v[24:27], v[120:123], v[92:95], v[24:27]\n" \
  "s_waitcnt lgkmcnt(12)\n" \
  "ds_read_b64_tr_b16 v[120:121], v146 offset:512\n" \
  "ds_read_b128 v[68:71], v150 offset:4096\n" \
  "v_mfma_f32_16x16x32_bf16 v[20:23], v[124:127], v[88:91], v[20:23]\n" \
  "s_and_b32 s52, s50, 0x3c000\n" \
  "ds_read_b64_tr_b16 v[122:123], v146 offset:768\n" \
  "v_mfma_f32_16x16x32_bf16 v[28:31], v[124:127], v[92:95], v[28:31]\n" \
  "s_waitcnt lgkmcnt(13)\n" \
  "ds_read_b64_tr_b16 v[124:125], v147 offset:512\n" \
  "v_mfma_f32_16x16x32_bf16 v[32:35], v[128:131], v[88:91], v[32:35]\n" \
  "ds_read_b64_tr_b16 v[126:127], v147 offset:768\n" \
  "v_add_u32 v148, s52, v144\n" \
  "v_mfma_f32_16x16x32_bf16 v[40:43], v[128:131], v[92:95], v[40:43]\n" \
  "s_waitcnt lgkmcnt(12)\n" \
  "ds_read_b64_tr_b16 v[128:129], v146 offset:1024\n" \
  "ds_read_b128 v[72:75], v150 offset:512\n" \
  "v_mfma_f32_16x16x32_bf16 v[36:39], v[132:135], v[88:91], v[36:39]\n" \
  "ds_read_b64_tr_b16 v[130:131], v146 offset:1280\n" \
  "v_xor_b32 v149, 32, v148\n" \
  "v_mfma_f32_16x16x32_bf16 v[44:47], v[132:135], v[92:95], v[44:47]\n" \
  "ds_read_b64_tr_b16 v[132:133], v147 offset:1024\n" \
  "v_mfma_f32_16x16x32_bf16 v[48:51], v[136:139], v[88:91], v[48:51]\n" \
  "ds_read_b64_tr_b16 v[134:135], v147 offset:1280\n" \
  "v_mfma_f32_16x16x32_bf16 v[56:59], v[136:139], v[92:95], v[56:59]\n" \
  "s_waitcnt lgkmcnt(12)\n" \
  "ds_read_b64_tr_b16 v[136:137], v146 offset:1536\n" \
  "ds_read_b128 v[76:79], v150 offset:4608\n" \
  "v_mfma_f32_16x16x32_bf16 v[52:55], v[140:143], v[88:91], v[52:55]\n" \
  "s_cmp_lt_u32 s54, 20\n" \
  "ds_read_b64_tr_b16 v[138:139], v146 offset:1792\n" \
  "v_mfma_f32_16x16x32_bf16 v[60:63], v[140:143], v[92:95], v[60:63]\n" \
  "ds_read_b64_tr_b16 v[140:141], v147 offset:1536\n" \
  "ds_read_b64_tr_b16 v[142:143], v147 offset:1792\n" \
  "s_cbranch_scc1 .Lqloop_%=\n" \
  "s_waitcnt lgkmcnt(0)\n" \
  "v_readlane_b32 s50, v160, 23\n" \
  "s_mov_b32 s64, 0x3e0293ee\n" \
  "s_mov_b32 s65, 0x3e0293ee\n" \
  "v_mov_b64 v[152:153], 0\n" \
  "v_mov_b64 v[154:155], 0\n" \
  "v_mov_b64 v[156:157], 0\n" \
  "v_mov_b64 v[158:159], 0\n" \
  "v_pk_mul_f32 v[0:1], s[64:65], v[0:1]\n" \
  "v_pk_mul_f32 v[2:3], s[64:65], v[2:3]\n" \
  "v_pk_mul_f32 v[4:5], s[64:65], v[4:5]\n" \
  "v_pk_mul_f32 v[6:7], s[64:65], v[6:7]\n" \
  "v_pk_mul_f32 v[8:9], s[64:65], v[8:9]\n" \
  "v_pk_mul_f32 v[10:11], s[64:65], v[10:11]\n" \
  "v_pk_mul_f32 v[12:13], s[64:65], v[12:13]\n" \
  "v_pk_mul_f32 v[14:15], s[64:65], v[14:15]\n" \
  "v_pk_mul_f32 v[16:17], s[64:65], v[16:17]\n" \
  "v_pk_mul_f32 v[18:19], s[64:65], v[18:19]\n" \
  "v_pk_mul_f32 v[20:21], s[64:65], v[20:21]\n" \
  "v_pk_mul_f32 v[22:23], s[64:65], v[22:23]\n" \
  "v_pk_mul_f32 v[24:25], s[64:65], v[24:25]\n" \
  "v_pk_mul_f32 v[26:27], s[64:65], v[26:27]\n" \
  "v_pk_mul_f32 v[28:29], s[64:65], v[28:29]\n" \
  "v_pk_mul_f32 v[30:31], s[64:65], v[30:31]\n" \
  "v_pk_mul_f32 v[32:33], s[64:65], v[32:33]\n" \
  "v_pk_mul_f32 v[34:35], s[64:65], v[34:35]\n" \
  "v_pk_mul_f32 v[36:37], s[64:65], v[36:37]\n" \
  "v_pk_mul_f32 v[38:39], s[64:65], v[38:39]\n" \
  "v_pk_mul_f32 v[40:41], s[64:65], v[40:41]\n" \
  "v_pk_mul_f32 v[42:43], s[64:65], v[42:43]\n" \
  "v_pk_mul_f32 v[44:45], s[64:65], v[44:45]\n" \
  "v_pk_mul_f32 v[46:47], s[64:65], v[46:47]\n" \
  "v_pk_mul_f32 v[48:49], s[64:65], v[48:49]\n" \
  "v_pk_mul_f32 v[50:51], s[64:65], v[50:51]\n" \
  "v_pk_mul_f32 v[52:53], s[64:65], v[52:53]\n" \
  "v_pk_mul_f32 v[54:55], s[64:65], v[54:55]\n" \
  "v_pk_mul_f32 v[56:57], s[64:65], v[56:57]\n" \
  "v_pk_mul_f32 v[58:59], s[64:65], v[58:59]\n" \
  "v_pk_mul_f32 v[60:61], s[64:65], v[60:61]\n" \
  "v_pk_mul_f32 v[62:63], s[64:65], v[62:63]\n" \
  "v_cvt_pk_bf16_f32 v64, v0, v1\n" \
  "v_cvt_pk_bf16_f32 v65, v2, v3\n" \
  "v_cvt_pk_bf16_f32 v66, v4, v5\n" \
  "v_cvt_pk_bf16_f32 v67, v6, v7\n" \
  "v_mfma_f32_16x16x32_bf16 v[0:3], v[152:155], v[152:155], 0\n" \
  "v_cvt_pk_bf16_f32 v68, v8, v9\n" \
  "v_cvt_pk_bf16_f32 v69, v10, v11\n" \
  "v_cvt_pk_bf16_f32 v70, v12, v13\n" \
  "v_cvt_pk_bf16_f32 v71, v14, v15\n" \
  "v_mfma_f32_16x16x32_bf16 v[4:7], v[152:155], v[152:155], 0\n" \
  "v_cvt_pk_bf16_f32 v72, v16, v17\n" \
  "v_cvt_pk_bf16_f32 v73, v18, v19\n" \
  "v_cvt_pk_bf16_f32 v74, v20, v21\n" \
  "v_cvt_pk_bf16_f32 v75, v22, v23\n" \
  "v_mfma_f32_16x16x32_bf16 v[8:11], v[152:155], v[152:155], 0\n" \
  "v_cvt_pk_bf16_f32 v76, v24, v25\n" \
  "v_cvt_pk_bf16_f32 v77, v26, v27\n" \
  "v_cvt_pk_bf16_f32 v78, v28, v29\n" \
  "v_cvt_pk_bf16_f32 v79, v30, v31\n" \
  "v_mfma_f32_16x16x32_bf16 v[12:15], v[152:155], v[152:155], 0\n" \
  "v_cvt_pk_bf16_f32 v80, v32, v33\n" \
  "v_cvt_pk_bf16_f32 v81, v34, v35\n" \
  "v_cvt_pk_bf16_f32 v82, v36, v37\n" \
  "v_cvt_pk_bf16_f32 v83, v38, v39\n" \
  "v_mfma_f32_16x16x32_bf16 v[16:19], v[152:155], v[152:155], 0\n" \
  "v_cvt_pk_bf16_f32 v84, v40, v41\n" \
  "v_cvt_pk_bf16_f32 v85, v42, v43\n" \
  "v_cvt_pk_bf16_f32 v86, v44, v45\n" \
  "v_cvt_pk_bf16_f32 v87, v46, v47\n" \
  "v_mfma_f32_16x16x32_bf16 v[20:23], v[152:155], v[152:155], 0\n" \
  "v_cvt_pk_bf16_f32 v88, v48, v49\n" \
  "v_cvt_pk_bf16_f32 v89, v50, v51\n" \
  "v_cvt_pk_bf16_f32 v90, v52, v53\n" \
  "v_cvt_pk_bf16_f32 v91, v54, v55\n" \
  "v_mfma_f32_16x16x32_bf16 v[24:27], v[152:155], v[152:155], 0\n" \
  "v_cvt_pk_bf16_f32 v92, v56, v57\n" \
  "v_cvt_pk_bf16_f32 v93, v58, v59\n" \
  "v_cvt_pk_bf16_f32 v94, v60, v61\n" \
  "v_cvt_pk_bf16_f32 v95, v62, v63\n" \
  "v_mfma_f32_16x16x32_bf16 v[28:31], v[152:155], v[152:155], 0\n" \
  "v_permlane32_swap_b32 v64, v66\n" \
  "v_permlane32_swap_b32 v65, v67\n" \
  "v_permlane32_swap_b32 v68, v70\n" \
  "v_permlane32_swap_b32 v69, v71\n" \
  "v_mfma_f32_16x16x32_bf16 v[32:35], v[152:155], v[152:155], 0\n" \
  "v_permlane32_swap_b32 v72, v74\n" \
  "v_permlane32_swap_b32 v73, v75\n" \
  "v_permlane32_swap_b32 v76, v78\n" \
  "v_permlane32_swap_b32 v77, v79\n" \
  "v_mfma_f32_16x16x32_bf16 v[36:39], v[152:155], v[152:155], 0\n" \
  "v_permlane32_swap_b32 v80, v82\n" \
  "v_permlane32_swap_b32 v81, v83\n" \
  "v_permlane32_swap_b32 v84, v86\n" \
  "v_permlane32_swap_b32 v85, v87\n" \
  "v_mfma_f32_16x16x32_bf16 v[40:43], v[152:155], v[152:155], 0\n" \
  "v_permlane32_swap_b32 v88, v90\n" \
  "v_permlane32_swap_b32 v89, v91\n" \
  "v_permlane32_swap_b32 v92, v94\n" \
  "v_permlane32_swap_b32 v93, v95\n" \
  "v_mfma_f32_16x16x32_bf16 v[44:47], v[152:155], v[152:155], 0\n" \
  "v_permlane16_swap_b32 v64, v66\n" \
  "v_permlane16_swap_b32 v65, v67\n" \
  "v_permlane16_swap_b32 v68, v70\n" \
  "v_permlane16_swap_b32 v69, v71\n" \
  "v_mfma_f32_16x16x32_bf16 v[48:51], v[152:155], v[152:155], 0\n" \
  "v_permlane16_swap_b32 v72, v74\n" \
  "v_permlane16_swap_b32 v73, v75\n" \
  "v_permlane16_swap_b32 v76, v78\n" \
  "v_permlane16_swap_b32 v77, v79\n" \
  "v_mfma_f32_16x16x32_bf16 v[52:55], v[152:155], v[152:155], 0\n" \
  "v_permlane16_swap_b32 v80, v82\n" \
  "v_permlane16_swap_b32 v81, v83\n" \
  "v_permlane16_swap_b32 v84, v86\n" \
  "v_permlane16_swap_b32 v85, v87\n" \
  "v_mfma_f32_16x16x32_bf16 v[56:59], v[152:155], v[152:155], 0\n" \
  "v_permlane16_swap_b32 v88, v90\n" \
  "v_permlane16_swap_b32 v89, v91\n" \
  "v_permlane16_swap_b32 v92, v94\n" \
  "v_permlane16_swap_b32 v93, v95\n" \
  "v_mfma_f32_16x16x32_bf16 v[60:63], v[152:155], v[152:155], 0\n" \
  "v_mov_b64 v[164:165], 0\n" \
  "s_mov_b32 s62, 0xff800000\n" \
  "s_mov_b32 s63, 0xff800000\n" \
  "s_and_b32 s52, s50, 0x3c000\n" \
  "s_mov_b32 s53, s52\n" \
  "v_add_u32 v161, s52, v161\n" \
  "s_waitcnt vmcnt(0)\n" \
  "s_cmp_lg_u32 s45, 3\n" \
  "s_cbranch_scc1 .Lnoch_%=\n" \
  "v_mbcnt_lo_u32_b32 v166, -1, 0\n" \
  "v_mbcnt_hi_u32_b32 v166, -1, v166\n" \
  "v_lshlrev_b32 v166, 4, v166\n" \
  "v_add_u32 v167, 1, v109\n" \
  "v_bfe_u32 v166, v166, 2, 7\n" \
  "global_store_dword v166, v167, s[36:37] offset:2048 sc1\n" \
  ".Lnoch_%=:\n" \
  "v_mov_b64 v[104:105], 0\n" \
  "v_mov_b64 v[106:107], 0\n" \
  "v_mov_b64 v[108:109], 0\n" \
  "v_mov_b64 v[110:111], 0\n" \
  "s_lshr_b32 s51, s50, 24\n" \
  "s_cbranch_scc1 .Lstub_a0_%=\n" \
  ".Lcont_a0_%=:\n" \
  "s_barrier\n" \
  "ds_read_b128 v[112:115], v161\n" \
  "ds_read_b128 v[116:119], v161 offset:512\n" \
  "ds_read_b128 v[120:123], v161 offset:1024\n" \
  "ds_read_b128 v[124:127], v161 offset:1536\n" \
  "ds_read_b128 v[128:131], v161 offset:4096\n" \
  "ds_read_b128 v[132:135], v161 offset:4608\n" \
  "ds_read_b128 v[136:139], v161 offset:5120\n" \
  "ds_read_b128 v[140:143], v161 offset:5632\n" \
  "s_waitcnt lgkmcnt(0)\n" \
  "s_mov_b32 s54, 24\n" \
  ".Laloop_%=:\n" \
  "s_waitcnt lgkmcnt(7)\n" \
  "v_mfma_f32_16x16x32_bf16 v[96:99], v[112:115], v[64:67], v[152:155]\n" \
  "s_waitcnt lgkmcnt(4)\n" \
  "v_add_f32 v165, v165, v104\n" \
  "v_add_f32 v165, v165, v105\n" \
  "v_mfma_f32_16x16x32_bf16 v[96:99], v[116:119], v[72:75], v[96:99]\n" \
  "v_add_f32 v165, v165, v106\n" \
  "v_add_f32 v165, v165, v107\n" \
  "v_mfma_f32_16x16x32_bf16 v[96:99], v[120:123], v[80:83], v[96:99]\n" \
  "v_add_f32 v165, v165, v108\n" \
  "v_add_f32 v165, v165, v109\n" \
  "v_mfma_f32_16x16x32_bf16 v[96:99], v[124:127], v[88:91], v[96:99]\n" \
  "s_waitcnt lgkmcnt(0)\n" \
  "v_add_f32 v165, v165, v110\n" \
  "v_add_f32 v165, v165, v111\n" \
  "v_mfma_f32_16x16x32_bf16 v[100:103], v[128:131], v[64:67], v[152:155]\n" \
  "v_add_u32 v162, s53, v162\n" \
  "v_readlane_b32 s50, v160, s54\n" \
  "v_mfma_f32_16x16x32_bf16 v[100:103], v[132:135], v[72:75], v[100:103]\n" \
  "s_add_u32 s54, s54, 1\n" \
  "v_xor_b32 v163, 32, v162\n" \
  "v_mfma_f32_16x16x32_bf16 v[100:103], v[136:139], v[80:83], v[100:103]\n" \
  "s_and_b32 s55, s50, 0x3c000\n" \
  "v_max3_f32 v166, v96, v97, v98\n" \
  "v_mfma_f32_16x16x32_bf16 v[100:103], v[140:143], v[88:91], v[100:103]\n" \
  "s_sub_u32 s53, s55, s52\n" \
  "v_max3_f32 v166, v166, v99, v99\n" \
  "v_mfma_f32_16x16x32_bf16 v[104:107], v[112:115], v[68:71], v[156:159]\n" \
  "s_mov_b32 s52, s55\n" \
  "ds_read_b64_tr_b16 v[112:113], v162 offset:8192\n" \
  "ds_read_b64_tr_b16 v[114:115], v162 offset:12288\n" \
  "v_mfma_f32_16x16x32_bf16 v[104:107], v[116:119], v[76:79], v[104:107]\n" \
  "ds_read_b64_tr_b16 v[116:117], v163 offset:8192\n" \
  "ds_read_b64_tr_b16 v[118:119], v163 offset:12288\n" \
  "v_mfma_f32_16x16x32_bf16 v[104:107], v[120:123], v[84:87], v[104:107]\n" \
  "ds_read_b64_tr_b16 v[120:121], v162 offset:8704\n" \
  "v_max3_f32 v167, v100, v101, v102\n" \
  "v_max3_f32 v166, v166, v167, v103\n" \
  "v_mfma_f32_16x16x32_bf16 v[104:107], v[124:127], v[92:95], v[104:107]\n" \
  "ds_read_b64_tr_b16 v[122:123], v162 offset:12800\n" \
  "v_cmp_nge_f32 vcc, s62, v166\n" \
  "v_add_u32 v161, s53, v161\n" \
  "v_mfma_f32_16x16x32_bf16 v[108:111], v[128:131], v[68:71], v[156:159]\n" \
  "s_cbranch_vccnz .Lresc0_%=\n" \
  ".Lresc_back0_%=:\n" \
  "ds_read_b64_tr_b16 v[124:125], v163 offset:8704\n" \
  "v_exp_f32 v96, v96\n" \
  "v_mfma_f32_16x16x32_bf16 v[108:111], v[132:135], v[76:79], v[108:111]\n" \
  "ds_read_b64_tr_b16 v[126:127], v163 offset:12800\n" \
  "v_exp_f32 v97, v97\n" \
  "v_exp_f32 v98, v98\n" \
  "v_mfma_f32_16x16x32_bf16 v[108:111], v[136:139], v[84:87], v[108:111]\n" \
  "ds_read_b64_tr_b16 v[128:129], v162 offset:9216\n" \
  "v_exp_f32 v99, v99\n" \
  "v_exp_f32 v100, v100\n" \
  "v_cvt_pk_bf16_f32 v144, v96, v97\n" \
  "v_mfma_f32_16x16x32_bf16 v[108:111], v[140:143], v[92:95], v[108:111]\n" \
  "ds_read_b64_tr_b16 v[130:131], v162 offset:13312\n" \
  "v_exp_f32 v101, v101\n" \
  "v_cvt_pk_bf16_f32 v145, v98, v99\n" \
  "v_exp_f32 v102, v102\n" \
  "v_exp_f32 v103, v103\n" \
  "v_cvt_pk_bf16_f32 v146, v100, v101\n" \
  "v_max3_f32 v166, v104, v105, v106\n" \
  "v_cvt_pk_bf16_f32 v147, v102, v103\n" \
  "v_max3_f32 v166, v166, v107, v107\n" \
  "s_waitcnt lgkmcnt(2)\n" \
  "s_nop 0\n" \
  "v_mfma_f32_16x16x32_bf16 v[0:3], v[112:115], v[144:147], v[0:3]\n" \
  "ds_read_b64_tr_b16 v[132:133], v163 offset:9216\n" \
  "ds_read_b64_tr_b16 v[134:135], v163 offset:13312\n" \
  "v_max3_f32 v167, v108, v109, v110\n" \
  "v_mfma_f32_16x16x32_bf16 v[4:7], v[116:119], v[144:147], v[4:7]\n" \
  "ds_read_b64_tr_b16 v[136:137], v162 offset:9728\n" \
  "v_max3_f32 v166, v166, v167, v111\n" \
  "v_mfma_f32_16x16x32_bf16 v[16:19], v[120:123], v[144:147], v[16:19]\n" \
  "ds_read_b64_tr_b16 v[138:139], v162 offset:13824\n" \
  "v_cmp_nge_f32 vcc, s62, v166\n" \
  "v_mfma_f32_16x16x32_bf16 v[20:23], v[124:127], v[144:147], v[20:23]\n" \
  "s_waitcnt lgkmcnt(4)\n" \
  "s_cbranch_vccnz .Lresc1_%=\n" \
  ".Lresc_back1_%=:\n" \
  "ds_read_b64_tr_b16 v[140:141], v163 offset:9728\n" \
  "v_exp_f32 v104, v104\n" \
  "v_mfma_f32_16x16x32_bf16 v[32:35], v[128:131], v[144:147], v[32:35]\n" \
  "s_waitcnt lgkmcnt(3)\n" \
  "ds_read_b64_tr_b16 v[142:143], v163 offset:13824\n" \
  "v_exp_f32 v105, v105\n" \
  "v_exp_f32 v106, v106\n" \
  "v_mfma_f32_16x16x32_bf16 v[36:39], v[132:135], v[144:147], v[36:39]\n" \
  "s_waitcnt lgkmcnt(2)\n" \
  "v_exp_f32 v107, v107\n" \
  "v_exp_f32 v108, v108\n" \
  "v_cvt_pk_bf16_f32 v148, v104, v105\n" \
  "v_mfma_f32_16x16x32_bf16 v[48:51], v[136:139], v[144:147], v[48:51]\n" \
  "s_waitcnt lgkmcnt(0)\n" \
  "v_exp_f32 v109, v109\n" \
  "v_exp_f32 v110, v110\n" \
  "v_cvt_pk_bf16_f32 v149, v106, v107\n" \
  "v_mfma_f32_16x16x32_bf16 v[52:55], v[140:143], v[144:147], v[52:55]\n" \
  "s_lshr_b32 s51, s50, 24\n" \
  "v_exp_f32 v111, v111\n" \
  "v_cvt_pk_bf16_f32 v150, v108, v109\n" \
  "v_cvt_pk_bf16_f32 v151, v110, v111\n" \
  "v_add_f32 v164, v164, v96\n" \
  "s_cbranch_scc1 .Lstub_kv_%=\n" \
  ".Lcont_kv_%=:\n" \
  "v_mfma_f32_16x16x32_bf16 v[8:11], v[112:115], v[148:151], v[8:11]\n" \
  "s_barrier\n" \
  "ds_read_b128 v[112:115], v161\n" \
  "v_add_f32 v164, v164, v97\n" \
  "v_mfma_f32_16x16x32_bf16 v[12:15], v[116:119], v[148:151], v[12:15]\n" \
  "ds_read_b128 v[116:119], v161 offset:512\n" \
  "v_add_f32 v164, v164, v98\n" \
  "v_mfma_f32_16x16x32_bf16 v[24:27], v[120:123], v[148:151], v[24:27]\n" \
  "ds_read_b128 v[120:123], v161 offset:1024\n" \
  "v_add_f32 v164, v164, v99\n" \
  "v_mfma_f32_16x16x32_bf16 v[28:31], v[124:127], v[148:151], v[28:31]\n" \
  "ds_read_b128 v[124:127], v161 offset:1536\n" \
  "v_add_f32 v164, v164, v100\n" \
  "v_mfma_f32_16x16x32_bf16 v[40:43], v[128:131], v[148:151], v[40:43]\n" \
  "ds_read_b128 v[128:131], v161 offset:4096\n" \
  "v_add_f32 v164, v164, v101\n" \
  "v_mfma_f32_16x16x32_bf16 v[44:47], v[132:135], v[148:151], v[44:47]\n" \
  "ds_read_b128 v[132:135], v161 offset:4608\n" \
  "v_add_f32 v164, v164, v102\n" \
  "v_mfma_f32_16x16x32_bf16 v[56:59], v[136:139], v[148:151], v[56:59]\n" \
  "s_cmp_lt_u32 s54, 40\n" \
  "ds_read_b128 v[136:139], v161 offset:5120\n" \
  "v_add_f32 v164, v164, v103\n" \
  "v_mfma_f32_16x16x32_bf16 v[60:63], v[140:143], v[148:151], v[60:63]\n" \
  "ds_read_b128 v[140:143], v161 offset:5632\n" \
  "s_cbranch_scc1 .Laloop_%=\n" \
  "s_waitcnt vmcnt(0)\n" \
  "v_add_f32 v165, v165, v104\n" \
  "v_add_f32 v165, v165, v105\n" \
  "v_add_f32 v165, v165, v106\n" \
  "v_add_f32 v165, v165, v107\n" \
  "v_add_f32 v165, v165, v108\n" \
  "v_add_f32 v165, v165, v109\n" \
  "v_add_f32 v165, v165, v110\n" \
  "v_add_f32 v165, v165, v111\n" \
  "v_bfe_u32 v166, v160, 18, 4\n" \
  "v_lshlrev_b32 v160, 18, v160\n" \
  "v_and_b32 v160, 0xff000000, v160\n" \
  "v_lshl_or_b32 v160, v166, 14, v160\n" \
  "s_sub_u32 s53, s53, s52\n" \
  "v_add_u32 v162, s53, v162\n" \
  "s_lshr_b32 s55, s45, 1\n" \
  "v_readlane_b32 s50, v160, s55\n" \
  "v_readlane_b32 s59, v160, 0\n" \
  "v_mov_b32 v96, v164\n" \
  "v_mov_b32 v97, v165\n" \
  "s_nop 0\n" \
  "v_permlane32_swap_b32 v164, v96\n" \
  "v_permlane32_swap_b32 v165, v97\n" \
  "v_add_f32 v164, v164, v96\n" \
  "v_add_f32 v165, v165, v97\n" \
  "v_mov_b32 v96, v164\n" \
  "v_mov_b32 v97, v165\n" \
  "v_mbcnt_lo_u32_b32 v167, -1, 0\n" \
  "v_mbcnt_hi_u32_b32 v167, -1, v167\n" \
  "v_lshlrev_b32 v167, 4, v167\n" \
  "v_permlane16_swap_b32 v164, v96\n" \
  "v_permlane16_swap_b32 v165, v97\n" \
  "v_add_f32 v164, v164, v96\n" \
  "v_add_f32 v165, v165, v97\n" \
  "v_rcp_f32 v144, v164\n" \
  "v_rcp_f32 v146, v165\n" \
  "s_and_b32 s52, s50, 0x3c000\n" \
  "s_add_u32 s55, s52, s41\n" \
  "s_add_u32 s55, s55, s44\n" \
  "s_waitcnt lgkmcnt(0)\n" \
  "s_xor_b32 s56, s55, 0\n" \
  "v_add_u32 v100, s56, v167\n" \
  "s_xor_b32 s56, s55, 2048\n" \
  "v_add_u32 v101, s56, v167\n" \
  "s_xor_b32 s56, s55, 4096\n" \
  "v_add_u32 v102, s56, v167\n" \
  "s_xor_b32 s56, s55, 6144\n" \
  "v_add_u32 v103, s56, v167\n" \
  "v_pk_mul_f32 v[0:1], v[144:145], v[0:1] op_sel_hi:[0,1]\n" \
  "v_pk_mul_f32 v[2:3], v[144:145], v[2:3] op_sel_hi:[0,1]\n" \
  "v_pk_mul_f32 v[4:5], v[144:145], v[4:5] op_sel_hi:[0,1]\n" \
  "v_pk_mul_f32 v[6:7], v[144:145], v[6:7] op_sel_hi:[0,1]\n" \
  "v_cvt_pk_bf16_f32 v112, v0, v1\n" \
  "v_cvt_pk_bf16_f32 v113, v2, v3\n" \
  "v_cvt_pk_bf16_f32 v114, v4, v5\n" \
  "v_cvt_pk_bf16_f32 v115, v6, v7\n" \
  "ds_write_b128 v100, v[112:115]\n" \
  "v_pk_mul_f32 v[8:9], v[146:147], v[8:9] op_sel_hi:[0,1]\n" \
  "v_pk_mul_f32 v[10:11], v[146:147], v[10:11] op_sel_hi:[0,1]\n" \
  "v_pk_mul_f32 v[12:13], v[146:147], v[12:13] op_sel_hi:[0,1]\n" \
  "v_pk_mul_f32 v[14:15], v[146:147], v[14:15] op_sel_hi:[0,1]\n" \
  "v_cvt_pk_bf16_f32 v116, v8, v9\n" \
  "v_cvt_pk_bf16_f32 v117, v10, v11\n" \
  "v_cvt_pk_bf16_f32 v118, v12, v13\n" \
  "v_cvt_pk_bf16_f32 v119, v14, v15\n" \
  "ds_write_b128 v100, v[116:119] offset:1024\n" \
  "v_pk_mul_f32 v[16:17], v[144:145], v[16:17] op_sel_hi:[0,1]\n" \
  "v_pk_mul_f32 v[18:19], v[144:145], v[18:19] op_sel_hi:[0,1]\n" \
  "v_pk_mul_f32 v[20:21], v[144:145], v[20:21] op_sel_hi:[0,1]\n" \
  "v_pk_mul_f32 v[22:23], v[144:145], v[22:23] op_sel_hi:[0,1]\n" \
  "v_cvt_pk_bf16_f32 v120, v16, v17\n" \
  "v_cvt_pk_bf16_f32 v121, v18, v19\n" \
  "v_cvt_pk_bf16_f32 v122, v20, v21\n" \
  "v_cvt_pk_bf16_f32 v123, v22, v23\n" \
  "ds_write_b128 v101, v[120:123]\n" \
  "v_pk_mul_f32 v[24:25], v[146:147], v[24:25] op_sel_hi:[0,1]\n" \
  "v_pk_mul_f32 v[26:27], v[146:147], v[26:27] op_sel_hi:[0,1]\n" \
  "v_pk_mul_f32 v[28:29], v[146:147], v[28:29] op_sel_hi:[0,1]\n" \
  "v_pk_mul_f32 v[30:31], v[146:147], v[30:31] op_sel_hi:[0,1]\n" \
  "v_cvt_pk_bf16_f32 v124, v24, v25\n" \
  "v_cvt_pk_bf16_f32 v125, v26, v27\n" \
  "v_cvt_pk_bf16_f32 v126, v28, v29\n" \
  "v_cvt_pk_bf16_f32 v127, v30, v31\n" \
  "ds_write_b128 v101, v[124:127] offset:1024\n" \
  "v_pk_mul_f32 v[32:33], v[144:145], v[32:33] op_sel_hi:[0,1]\n" \
  "v_pk_mul_f32 v[34:35], v[144:145], v[34:35] op_sel_hi:[0,1]\n" \
  "v_pk_mul_f32 v[36:37], v[144:145], v[36:37] op_sel_hi:[0,1]\n" \
  "v_pk_mul_f32 v[38:39], v[144:145], v[38:39] op_sel_hi:[0,1]\n" \
  "v_cvt_pk_bf16_f32 v128, v32, v33\n" \
  "v_cvt_pk_bf16_f32 v129, v34, v35\n" \
  "v_cvt_pk_bf16_f32 v130, v36, v37\n" \
  "v_cvt_pk_bf16_f32 v131, v38, v39\n" \
  "ds_write_b128 v102, v[128:131]\n" \
  "v_pk_mul_f32 v[40:41], v[146:147], v[40:41] op_sel_hi:[0,1]\n" \
  "v_pk_mul_f32 v[42:43], v[146:147], v[42:43] op_sel_hi:[0,1]\n" \
  "v_pk_mul_f32 v[44:45], v[146:147], v[44:45] op_sel_hi:[0,1]\n" \
  "v_pk_mul_f32 v[46:47], v[146:147], v[46:47] op_sel_hi:[0,1]\n" \
  "v_cvt_pk_bf16_f32 v132, v40, v41\n" \
  "v_cvt_pk_bf16_f32 v133, v42, v43\n" \
  "v_cvt_pk_bf16_f32 v134, v44, v45\n" \
  "v_cvt_pk_bf16_f32 v135, v46, v47\n" \
  "ds_write_b128 v102, v[132:135] offset:1024\n" \
  "v_pk_mul_f32 v[48:49], v[144:145], v[48:49] op_sel_hi:[0,1]\n" \
  "v_pk_mul_f32 v[50:51], v[144:145], v[50:51] op_sel_hi:[0,1]\n" \
  "v_pk_mul_f32 v[52:53], v[144:145], v[52:53] op_sel_hi:[0,1]\n" \
  "v_pk_mul_f32 v[54:55], v[144:145], v[54:55] op_sel_hi:[0,1]\n" \
  "v_cvt_pk_bf16_f32 v136, v48, v49\n" \
  "v_cvt_pk_bf16_f32 v137, v50, v51\n" \
  "v_cvt_pk_bf16_f32 v138, v52, v53\n" \
  "v_cvt_pk_bf16_f32 v139, v54, v55\n" \
  "ds_write_b128 v103, v[136:139]\n" \
  "v_pk_mul_f32 v[56:57], v[146:147], v[56:57] op_sel_hi:[0,1]\n" \
  "v_pk_mul_f32 v[58:59], v[146:147], v[58:59] op_sel_hi:[0,1]\n" \
  "v_pk_mul_f32 v[60:61], v[146:147], v[60:61] op_sel_hi:[0,1]\n" \
  "v_pk_mul_f32 v[62:63], v[146:147], v[62:63] op_sel_hi:[0,1]\n" \
  "v_cvt_pk_bf16_f32 v140, v56, v57\n" \
  "v_cvt_pk_bf16_f32 v141, v58, v59\n" \
  "v_cvt_pk_bf16_f32 v142, v60, v61\n" \
  "v_cvt_pk_bf16_f32 v143, v62, v63\n" \
  "ds_write_b128 v103, v[140:143] offset:1024\n" \
  "s_cmp_lg_u32 s42, 0\n" \
  "s_cbranch_scc1 .Lend1_%=\n" \
  "s_lshr_b32 s55, s45, 1\n" \
  "s_mul_i32 s55, s55, 6144\n" \
  "s_lshl_b32 s56, s45, 10\n" \
  "s_add_u32 s55, s55, s56\n" \
  "v_add_u32 v162, s55, v162\n" \
  "s_mov_b32 s54, 7\n" \
  "s_mov_b32 s58, 0\n" \
  "v_readlane_b32 s56, v160, 5\n" \
  "v_readlane_b32 s50, v160, 6\n" \
  "v_mov_b64 v[0:1], 0\n" \
  "v_mov_b64 v[2:3], 0\n" \
  "v_mov_b64 v[4:5], 0\n" \
  "s_nop 0\n" \
  "v_mfma_f32_16x16x32_bf16 v[48:51], v[0:3], v[0:3], 0\n" \
  "v_mov_b64 v[6:7], 0\n" \
  "v_mov_b64 v[8:9], 0\n" \
  "v_mfma_f32_16x16x32_bf16 v[52:55], v[0:3], v[0:3], 0\n" \
  "v_mov_b64 v[10:11], 0\n" \
  "v_mov_b64 v[12:13], 0\n" \
  "v_mfma_f32_16x16x32_bf16 v[56:59], v[0:3], v[0:3], 0\n" \
  "v_mov_b64 v[14:15], 0\n" \
  "v_mov_b64 v[16:17], 0\n" \
  "v_mfma_f32_16x16x32_bf16 v[60:63], v[0:3], v[0:3], 0\n" \
  "v_mov_b64 v[18:19], 0\n" \
  "v_mov_b64 v[20:21], 0\n" \
  "v_mfma_f32_16x16x32_bf16 v[64:67], v[0:3], v[0:3], 0\n" \
  "v_mov_b64 v[22:23], 0\n" \
  "v_mov_b64 v[24:25], 0\n" \
  "v_mfma_f32_16x16x32_bf16 v[68:71], v[0:3], v[0:3], 0\n" \
  "v_mov_b64 v[26:27], 0\n" \
  "v_mov_b64 v[28:29], 0\n" \
  "v_mfma_f32_16x16x32_bf16 v[72:75], v[0:3], v[0:3], 0\n" \
  "v_mov_b64 v[30:31], 0\n" \
  "v_mov_b64 v[32:33], 0\n" \
  "v_mfma_f32_16x16x32_bf16 v[76:79], v[0:3], v[0:3], 0\n" \
  "v_mov_b64 v[34:35], 0\n" \
  "v_mov_b64 v[36:37], 0\n" \
  "v_mfma_f32_16x16x32_bf16 v[80:83], v[0:3], v[0:3], 0\n" \
  "v_mov_b64 v[38:39], 0\n" \
  "v_mov_b64 v[40:41], 0\n" \
  "v_mfma_f32_16x16x32_bf16 v[84:87], v[0:3], v[0:3], 0\n" \
  "v_mov_b64 v[42:43], 0\n" \
  "v_mov_b64 v[44:45], 0\n" \
  "v_mfma_f32_16x16x32_bf16 v[88:91], v[0:3], v[0:3], 0\n" \
  "v_mov_b64 v[46:47], 0\n" \
  "v_mfma_f32_16x16x32_bf16 v[92:95], v[0:3], v[0:3], 0\n" \
  "s_and_b32 s55, s59, 0x3c000\n" \
  "v_add_u32 v164, s55, v167\n" \
  "s_and_b32 s52, s56, 0x3c000\n" \
  "v_add_u32 v161, s52, v162\n" \
  "v_xor_b32 v163, 32, v161\n" \
  "s_and_b32 s52, s50, 0x3c000\n" \
  "v_and_b32 v165, 0x70, v167\n" \
  "v_lshlrev_b32 v165, 1, v165\n" \
  "s_waitcnt lgkmcnt(0)\n" \
  "global_load_dword v166, v165, s[38:39] offset:2048 sc1\n" \
  "s_lshr_b32 s51, s56, 24\n" \
  "s_cbranch_scc1 .Lstub_o0p_%=\n" \
  ".Lcont_o0p_%=:\n" \
  "s_barrier\n" \
  "ds_read_b128 v[144:147], v164\n" \
  "ds_read_b128 v[148:151], v164 offset:1024\n" \
  "ds_read_b128 v[152:155], v164 offset:8192\n" \
  "ds_read_b128 v[156:159], v164 offset:9216\n" \
  "ds_read_b64_tr_b16 v[128:129], v161\n" \
  "ds_read_b64_tr_b16 v[130:131], v161 offset:4096\n" \
  "ds_read_b64_tr_b16 v[132:133], v163\n" \
  "ds_read_b64_tr_b16 v[134:135], v163 offset:4096\n" \
  "ds_read_b64_tr_b16 v[136:137], v161 offset:512\n" \
  "ds_read_b64_tr_b16 v[138:139], v161 offset:4608\n" \
  "v_mov_b64 v[96:97], 0\n" \
  "v_mov_b64 v[98:99], 0\n" \
  "v_mov_b64 v[100:101], 0\n" \
  "v_mov_b64 v[102:103], 0\n" \
  "v_mov_b64 v[104:105], 0\n" \
  "v_mov_b64 v[106:107], 0\n" \
  "v_mov_b64 v[108:109], 0\n" \
  "v_mov_b64 v[110:111], 0\n" \
  "v_mov_b64 v[112:113], 0\n" \
  "v_mov_b64 v[114:115], 0\n" \
  "v_mov_b64 v[116:117], 0\n" \
  "v_mov_b64 v[118:119], 0\n" \
  "v_mov_b64 v[120:121], 0\n" \
  "v_mov_b64 v[122:123], 0\n" \
  "v_mov_b64 v[124:125], 0\n" \
  "v_mov_b64 v[126:127], 0\n" \
  "s_waitcnt lgkmcnt(0)\n" \
  ".Loloop_%=:\n" \
  "s_waitcnt lgkmcnt(6)\n" \
  "v_mfma_f32_16x16x32_bf16 v[0:3], v[128:131], v[144:147], v[0:3]\n" \
  "s_waitcnt lgkmcnt(5)\n" \
  "ds_read_b64_tr_b16 v[140:141], v163 offset:512\n" \
  "v_mfma_f32_16x16x32_bf16 v[4:7], v[132:135], v[144:147], v[4:7]\n" \
  "s_waitcnt lgkmcnt(5)\n" \
  "ds_read_b64_tr_b16 v[142:143], v163 offset:4608\n" \
  "v_mfma_f32_16x16x32_bf16 v[8:11], v[128:131], v[148:151], v[8:11]\n" \
  "v_add_u32 v161, s52, v162\n" \
  "v_mfma_f32_16x16x32_bf16 v[12:15], v[132:135], v[148:151], v[12:15]\n" \
  "s_waitcnt lgkmcnt(5)\n" \
  "v_readlane_b32 s50, v160, s54\n" \
  "v_mfma_f32_16x16x32_bf16 v[16:19], v[128:131], v[152:155], v[16:19]\n" \
  "v_xor_b32 v163, 32, v161\n" \
  "v_mfma_f32_16x16x32_bf16 v[20:23], v[132:135], v[152:155], v[20:23]\n" \
  "s_waitcnt lgkmcnt(0)\n" \
  "s_barrier\n" \
  "v_mfma_f32_16x16x32_bf16 v[24:27], v[128:131], v[156:159], v[24:27]\n" \
  "ds_read_b64_tr_b16 v[128:129], v161\n" \
  "v_mfma_f32_16x16x32_bf16 v[28:31], v[132:135], v[156:159], v[28:31]\n" \
  "ds_read_b64_tr_b16 v[130:131], v161 offset:4096\n" \
  "v_mfma_f32_16x16x32_bf16 v[32:35], v[136:139], v[144:147], v[32:35]\n" \
  "s_add_u32 s54, s54, 1\n" \
  "ds_read_b64_tr_b16 v[132:133], v163\n" \
  "v_mfma_f32_16x16x32_bf16 v[36:39], v[140:143], v[144:147], v[36:39]\n" \
  "v_mfma_f32_16x16x32_bf16 v[40:43], v[136:139], v[148:151], v[40:43]\n" \
  "ds_read_b64_tr_b16 v[134:135], v163 offset:4096\n" \
  "v_mfma_f32_16x16x32_bf16 v[44:47], v[140:143], v[148:151], v[44:47]\n" \
  "v_mfma_f32_16x16x32_bf16 v[48:51], v[136:139], v[152:155], v[48:51]\n" \
  "s_and_b32 s52, s50, 0x3c000\n" \
  "v_mfma_f32_16x16x32_bf16 v[52:55], v[140:143], v[152:155], v[52:55]\n" \
  "v_mfma_f32_16x16x32_bf16 v[56:59], v[136:139], v[156:159], v[56:59]\n" \
  "ds_read_b64_tr_b16 v[136:137], v161 offset:512\n" \
  "v_mfma_f32_16x16x32_bf16 v[60:63], v[140:143], v[156:159], v[60:63]\n" \
  "s_waitcnt lgkmcnt(3)\n" \
  "ds_read_b64_tr_b16 v[138:139], v161 offset:4608\n" \
  "v_mfma_f32_16x16x32_bf16 v[64:67], v[128:131], v[144:147], v[64:67]\n" \
  "s_waitcnt lgkmcnt(2)\n" \
  "ds_read_b64_tr_b16 v[140:141], v163 offset:512\n" \
  "v_mfma_f32_16x16x32_bf16 v[68:71], v[132:135], v[144:147], v[68:71]\n" \
  "ds_read_b64_tr_b16 v[142:143], v163 offset:4608\n" \
  "v_mfma_f32_16x16x32_bf16 v[72:75], v[128:131], v[148:151], v[72:75]\n" \
  "v_add_u32 v161, s52, v162\n" \
  "v_mfma_f32_16x16x32_bf16 v[76:79], v[132:135], v[148:151], v[76:79]\n" \
  "v_readlane_b32 s50, v160, s54\n" \
  "v_mfma_f32_16x16x32_bf16 v[80:83], v[128:131], v[152:155], v[80:83]\n" \
  "v_xor_b32 v163, 32, v161\n" \
  "v_mfma_f32_16x16x32_bf16 v[84:87], v[132:135], v[152:155], v[84:87]\n" \
  "v_mfma_f32_16x16x32_bf16 v[88:91], v[128:131], v[156:159], v[88:91]\n" \
  "ds_read_b64_tr_b16 v[128:129], v161\n" \
  "v_mfma_f32_16x16x32_bf16 v[92:95], v[132:135], v[156:159], v[92:95]\n" \
  "s_waitcnt lgkmcnt(3)\n" \
  "ds_read_b64_tr_b16 v[130:131], v161 offset:4096\n" \
  "v_mfma_f32_16x16x32_bf16 v[96:99], v[136:139], v[144:147], v[96:99]\n" \
  "s_waitcnt lgkmcnt(2)\n" \
  "s_add_u32 s54, s54, 1\n" \
  "ds_read_b64_tr_b16 v[132:133], v163\n" \
  "v_mfma_f32_16x16x32_bf16 v[100:103], v[140:143], v[144:147], v[100:103]\n" \
  "ds_read_b128 v[144:147], v164 offset:2048\n" \
  "v_mfma_f32_16x16x32_bf16 v[104:107], v[136:139], v[148:151], v[104:107]\n" \
  "ds_read_b64_tr_b16 v[134:135], v163 offset:4096\n" \
  "v_mfma_f32_16x16x32_bf16 v[108:111], v[140:143], v[148:151], v[108:111]\n" \
  "ds_read_b128 v[148:151], v164 offset:3072\n" \
  "v_mfma_f32_16x16x32_bf16 v[112:115], v[136:139], v[152:155], v[112:115]\n" \
  "s_and_b32 s52, s50, 0x3c000\n" \
  "v_mfma_f32_16x16x32_bf16 v[116:119], v[140:143], v[152:155], v[116:119]\n" \
  "ds_read_b128 v[152:155], v164 offset:10240\n" \
  "v_mfma_f32_16x16x32_bf16 v[120:123], v[136:139], v[156:159], v[120:123]\n" \
  "ds_read_b64_tr_b16 v[136:137], v161 offset:512\n" \
  "v_mfma_f32_16x16x32_bf16 v[124:127], v[140:143], v[156:159], v[124:127]\n" \
  "s_waitcnt lgkmcnt(4)\n" \
  "ds_read_b128 v[156:159], v164 offset:11264\n" \
  "ds_read_b64_tr_b16 v[138:139], v161 offset:4608\n" \
  "v_mfma_f32_16x16x32_bf16 v[0:3], v[128:131], v[144:147], v[0:3]\n" \
  "s_waitcnt lgkmcnt(5)\n" \
  "ds_read_b64_tr_b16 v[140:141], v163 offset:512\n" \
  "v_mfma_f32_16x16x32_bf16 v[4:7], v[132:135], v[144:147], v[4:7]\n" \
  "s_waitcnt lgkmcnt(5)\n" \
  "ds_read_b64_tr_b16 v[142:143], v163 offset:4608\n" \
  "v_mfma_f32_16x16x32_bf16 v[8:11], v[128:131], v[148:151], v[8:11]\n" \
  "v_add_u32 v161, s52, v162\n" \
  "v_mfma_f32_16x16x32_bf16 v[12:15], v[132:135], v[148:151], v[12:15]\n" \
  "s_waitcnt lgkmcnt(5)\n" \
  "v_readlane_b32 s50, v160, s54\n" \
  "v_mfma_f32_16x16x32_bf16 v[16:19], v[128:131], v[152:155], v[16:19]\n" \
  "v_xor_b32 v163, 32, v161\n" \
  "v_mfma_f32_16x16x32_bf16 v[20:23], v[132:135], v[152:155], v[20:23]\n" \
  "s_waitcnt lgkmcnt(0)\n" \
  "s_barrier\n" \
  "v_mfma_f32_16x16x32_bf16 v[24:27], v[128:131], v[156:159], v[24:27]\n" \
  "ds_read_b64_tr_b16 v[128:129], v161\n" \
  "v_mfma_f32_16x16x32_bf16 v[28:31], v[132:135], v[156:159], v[28:31]\n" \
  "ds_read_b64_tr_b16 v[130:131], v161 offset:4096\n" \
  "v_mfma_f32_16x16x32_bf16 v[32:35], v[136:139], v[144:147], v[32:35]\n" \
  "s_add_u32 s54, s54, 1\n" \
  "ds_read_b64_tr_b16 v[132:133], v163\n" \
  "v_mfma_f32_16x16x32_bf16 v[36:39], v[140:143], v[144:147], v[36:39]\n" \
  "v_mfma_f32_16x16x32_bf16 v[40:43], v[136:139], v[148:151], v[40:43]\n" \
  "ds_read_b64_tr_b16 v[134:135], v163 offset:4096\n" \
  "v_mfma_f32_16x16x32_bf16 v[44:47], v[140:143], v[148:151], v[44:47]\n" \
  "v_mfma_f32_16x16x32_bf16 v[48:51], v[136:139], v[152:155], v[48:51]\n" \
  "s_and_b32 s52, s50, 0x3c000\n" \
  "v_mfma_f32_16x16x32_bf16 v[52:55], v[140:143], v[152:155], v[52:55]\n" \
  "v_mfma_f32_16x16x32_bf16 v[56:59], v[136:139], v[156:159], v[56:59]\n" \
  "ds_read_b64_tr_b16 v[136:137], v161 offset:512\n" \
  "v_mfma_f32_16x16x32_bf16 v[60:63], v[140:143], v[156:159], v[60:63]\n" \
  "s_waitcnt lgkmcnt(3)\n" \
  "ds_read_b64_tr_b16 v[138:139], v161 offset:4608\n" \
  "v_mfma_f32_16x16x32_bf16 v[64:67], v[128:131], v[144:147], v[64:67]\n" \
  "s_waitcnt lgkmcnt(2)\n" \
  "ds_read_b64_tr_b16 v[140:141], v163 offset:512\n" \
  "v_mfma_f32_16x16x32_bf16 v[68:71], v[132:135], v[144:147], v[68:71]\n" \
  "ds_read_b64_tr_b16 v[142:143], v163 offset:4608\n" \
  "v_mfma_f32_16x16x32_bf16 v[72:75], v[128:131], v[148:151], v[72:75]\n" \
  "v_add_u32 v161, s52, v162\n" \
  "v_mfma_f32_16x16x32_bf16 v[76:79], v[132:135], v[148:151], v[76:79]\n" \
  "v_readlane_b32 s50, v160, s54\n" \
  "v_mfma_f32_16x16x32_bf16 v[80:83], v[128:131], v[152:155], v[80:83]\n" \
  "v_xor_b32 v163, 32, v161\n" \
  "v_mfma_f32_16x16x32_bf16 v[84:87], v[132:135], v[152:155], v[84:87]\n" \
  "v_mfma_f32_16x16x32_bf16 v[88:91], v[128:131], v[156:159], v[88:91]\n" \
  "ds_read_b64_tr_b16 v[128:129], v161\n" \
  "v_mfma_f32_16x16x32_bf16 v[92:95], v[132:135], v[156:159], v[92:95]\n" \
  "s_waitcnt lgkmcnt(3)\n" \
  "ds_read_b64_tr_b16 v[130:131], v161 offset:4096\n" \
  "v_mfma_f32_16x16x32_bf16 v[96:99], v[136:139], v[144:147], v[96:99]\n" \
  "s_waitcnt lgkmcnt(2)\n" \
  "s_add_u32 s54, s54, 1\n" \
  "ds_read_b64_tr_b16 v[132:133], v163\n" \
  "v_mfma_f32_16x16x32_bf16 v[100:103], v[140:143], v[144:147], v[100:103]\n" \
  "ds_read_b128 v[144:147], v164 offset:4096\n" \
  "v_mfma_f32_16x16x32_bf16 v[104:107], v[136:139], v[148:151], v[104:107]\n" \
  "ds_read_b64_tr_b16 v[134:135], v163 offset:4096\n" \
  "v_mfma_f32_16x16x32_bf16 v[108:111], v[140:143], v[148:151], v[108:111]\n" \
  "ds_read_b128 v[148:151], v164 offset:5120\n" \
  "v_mfma_f32_16x16x32_bf16 v[112:115], v[136:139], v[152:155], v[112:115]\n" \
  "s_and_b32 s52, s50, 0x3c000\n" \
  "v_mfma_f32_16x16x32_bf16 v[116:119], v[140:143], v[152:155], v[116:119]\n" \
  "ds_read_b128 v[152:155], v164 offset:12288\n" \
  "v_mfma_f32_16x16x32_bf16 v[120:123], v[136:139], v[156:159], v[120:123]\n" \
  "ds_read_b64_tr_b16 v[136:137], v161 offset:512\n" \
  "v_mfma_f32_16x16x32_bf16 v[124:127], v[140:143], v[156:159], v[124:127]\n" \
  "s_waitcnt lgkmcnt(4)\n" \
  "ds_read_b128 v[156:159], v164 offset:13312\n" \
  "ds_read_b64_tr_b16 v[138:139], v161 offset:4608\n" \
  "v_mfma_f32_16x16x32_bf16 v[0:3], v[128:131], v[144:147], v[0:3]\n" \
  "s_waitcnt lgkmcnt(5)\n" \
  "ds_read_b64_tr_b16 v[140:141], v163 offset:512\n" \
  "v_mfma_f32_16x16x32_bf16 v[4:7], v[132:135], v[144:147], v[4:7]\n" \
  "s_waitcnt lgkmcnt(5)\n" \
  "ds_read_b64_tr_b16 v[142:143], v163 offset:4608\n" \
  "v_mfma_f32_16x16x32_bf16 v[8:11], v[128:131], v[148:151], v[8:11]\n" \
  "v_add_u32 v161, s52, v162\n" \
  "v_mfma_f32_16x16x32_bf16 v[12:15], v[132:135], v[148:151], v[12:15]\n" \
  "s_waitcnt lgkmcnt(5)\n" \
  "v_readlane_b32 s50, v160, s54\n" \
  "v_mfma_f32_16x16x32_bf16 v[16:19], v[128:131], v[152:155], v[16:19]\n" \
  "v_xor_b32 v163, 32, v161\n" \
  "v_mfma_f32_16x16x32_bf16 v[20:23], v[132:135], v[152:155], v[20:23]\n" \
  "s_waitcnt lgkmcnt(0)\n" \
  "s_barrier\n" \
  "v_mfma_f32_16x16x32_bf16 v[24:27], v[128:131], v[156:159], v[24:27]\n" \
  "ds_read_b64_tr_b16 v[128:129], v161\n" \
  "v_mfma_f32_16x16x32_bf16 v[28:31], v[132:135], v[156:159], v[28:31]\n" \
  "ds_read_b64_tr_b16 v[130:131], v161 offset:4096\n" \
  "v_mfma_f32_16x16x32_bf16 v[32:35], v[136:139], v[144:147], v[32:35]\n" \
  "s_add_u32 s54, s54, 1\n" \
  "ds_read_b64_tr_b16 v[132:133], v163\n" \
  "v_mfma_f32_16x16x32_bf16 v[36:39], v[140:143], v[144:147], v[36:39]\n" \
  "v_mfma_f32_16x16x32_bf16 v[40:43], v[136:139], v[148:151], v[40:43]\n" \
  "ds_read_b64_tr_b16 v[134:135], v163 offset:4096\n" \
  "v_mfma_f32_16x16x32_bf16 v[44:47], v[140:143], v[148:151], v[44:47]\n" \
  "v_mfma_f32_16x16x32_bf16 v[48:51], v[136:139], v[152:155], v[48:51]\n" \
  "s_and_b32 s52, s50, 0x3c000\n" \
  "v_mfma_f32_16x16x32_bf16 v[52:55], v[140:143], v[152:155], v[52:55]\n" \
  "v_mfma_f32_16x16x32_bf16 v[56:59], v[136:139], v[156:159], v[56:59]\n" \
  "ds_read_b64_tr_b16 v[136:137], v161 offset:512\n" \
  "v_mfma_f32_16x16x32_bf16 v[60:63], v[140:143], v[156:159], v[60:63]\n" \
  "s_waitcnt lgkmcnt(3)\n" \
  "ds_read_b64_tr_b16 v[138:139], v161 offset:4608\n" \
  "v_mfma_f32_16x16x32_bf16 v[64:67], v[128:131], v[144:147], v[64:67]\n" \
  "s_waitcnt lgkmcnt(2)\n" \
  "ds_read_b64_tr_b16 v[140:141], v163 offset:512\n" \
  "v_mfma_f32_16x16x32_bf16 v[68:71], v[132:135], v[144:147], v[68:71]\n" \
  "ds_read_b64_tr_b16 v[142:143], v163 offset:4608\n" \
  "v_mfma_f32_16x16x32_bf16 v[72:75], v[128:131], v[148:151], v[72:75]\n" \
  "v_add_u32 v161, s52, v162\n" \
  "v_mfma_f32_16x16x32_bf16 v[76:79], v[132:135], v[148:151], v[76:79]\n" \
  "v_readlane_b32 s50, v160, s54\n" \
  "v_mfma_f32_16x16x32_bf16 v[80:83], v[128:131], v[152:155], v[80:83]\n" \
  "v_xor_b32 v163, 32, v161\n" \
  "v_mfma_f32_16x16x32_bf16 v[84:87], v[132:135], v[152:155], v[84:87]\n" \
  "v_mfma_f32_16x16x32_bf16 v[88:91], v[128:131], v[156:159], v[88:91]\n" \
  "ds_read_b64_tr_b16 v[128:129], v161\n" \
  "v_mfma_f32_16x16x32_bf16 v[92:95], v[132:135], v[156:159], v[92:95]\n" \
  "s_waitcnt lgkmcnt(3)\n" \
  "ds_read_b64_tr_b16 v[130:131], v161 offset:4096\n" \
  "v_mfma_f32_16x16x32_bf16 v[96:99], v[136:139], v[144:147], v[96:99]\n" \
  "s_waitcnt lgkmcnt(2)\n" \
  "s_add_u32 s54, s54, 1\n" \
  "ds_read_b64_tr_b16 v[132:133], v163\n" \
  "v_mfma_f32_16x16x32_bf16 v[100:103], v[140:143], v[144:147], v[100:103]\n" \
  "ds_read_b128 v[144:147], v164 offset:6144\n" \
  "v_mfma_f32_16x16x32_bf16 v[104:107], v[136:139], v[148:151], v[104:107]\n" \
  "ds_read_b64_tr_b16 v[134:135], v163 offset:4096\n" \
  "v_mfma_f32_16x16x32_bf16 v[108:111], v[140:143], v[148:151], v[108:111]\n" \
  "ds_read_b128 v[148:151], v164 offset:7168\n" \
  "v_mfma_f32_16x16x32_bf16 v[112:115], v[136:139], v[152:155], v[112:115]\n" \
  "s_and_b32 s52, s50, 0x3c000\n" \
  "v_mfma_f32_16x16x32_bf16 v[116:119], v[140:143], v[152:155], v[116:119]\n" \
  "ds_read_b128 v[152:155], v164 offset:14336\n" \
  "v_mfma_f32_16x16x32_bf16 v[120:123], v[136:139], v[156:159], v[120:123]\n" \
  "ds_read_b64_tr_b16 v[136:137], v161 offset:512\n" \
  "v_mfma_f32_16x16x32_bf16 v[124:127], v[140:143], v[156:159], v[124:127]\n" \
  "s_waitcnt lgkmcnt(4)\n" \
  "ds_read_b128 v[156:159], v164 offset:15360\n" \
  "ds_read_b64_tr_b16 v[138:139], v161 offset:4608\n" \
  "v_mfma_f32_16x16x32_bf16 v[0:3], v[128:131], v[144:147], v[0:3]\n" \
  "s_waitcnt lgkmcnt(5)\n" \
  "ds_read_b64_tr_b16 v[140:141], v163 offset:512\n" \
  "v_mfma_f32_16x16x32_bf16 v[4:7], v[132:135], v[144:147], v[4:7]\n" \
  "s_waitcnt lgkmcnt(5)\n" \
  "ds_read_b64_tr_b16 v[142:143], v163 offset:4608\n" \
  "v_mfma_f32_16x16x32_bf16 v[8:11], v[128:131], v[148:151], v[8:11]\n" \
  "v_add_u32 v161, s52, v162\n" \
  "v_mfma_f32_16x16x32_bf16 v[12:15], v[132:135], v[148:151], v[12:15]\n" \
  "s_waitcnt lgkmcnt(5)\n" \
  "v_readlane_b32 s50, v160, s54\n" \
  "v_mfma_f32_16x16x32_bf16 v[16:19], v[128:131], v[152:155], v[16:19]\n" \
  "v_xor_b32 v163, 32, v161\n" \
  "v_mfma_f32_16x16x32_bf16 v[20:23], v[132:135], v[152:155], v[20:23]\n" \
  "s_waitcnt lgkmcnt(0)\n" \
  "s_barrier\n" \
  "v_mfma_f32_16x16x32_bf16 v[24:27], v[128:131], v[156:159], v[24:27]\n" \
  "ds_read_b64_tr_b16 v[128:129], v161\n" \
  "v_mfma_f32_16x16x32_bf16 v[28:31], v[132:135], v[156:159], v[28:31]\n" \
  "ds_read_b64_tr_b16 v[130:131], v161 offset:4096\n" \
  "v_mfma_f32_16x16x32_bf16 v[32:35], v[136:139], v[144:147], v[32:35]\n" \
  "s_add_u32 s54, s54, 1\n" \
  "ds_read_b64_tr_b16 v[132:133], v163\n" \
  "v_mfma_f32_16x16x32_bf16 v[36:39], v[140:143], v[144:147], v[36:39]\n" \
  "s_add_u32 s58, s58, 1\n" \
  "v_mfma_f32_16x16x32_bf16 v[40:43], v[136:139], v[148:151], v[40:43]\n" \
  "ds_read_b64_tr_b16 v[134:135], v163 offset:4096\n" \
  "v_mfma_f32_16x16x32_bf16 v[44:47], v[140:143], v[148:151], v[44:47]\n" \
  "v_readlane_b32 s59, v160, s58\n" \
  "v_mfma_f32_16x16x32_bf16 v[48:51], v[136:139], v[152:155], v[48:51]\n" \
  "s_and_b32 s52, s50, 0x3c000\n" \
  "v_mfma_f32_16x16x32_bf16 v[52:55], v[140:143], v[152:155], v[52:55]\n" \
  "v_mfma_f32_16x16x32_bf16 v[56:59], v[136:139], v[156:159], v[56:59]\n" \
  "ds_read_b64_tr_b16 v[136:137], v161 offset:512\n" \
  "v_mfma_f32_16x16x32_bf16 v[60:63], v[140:143], v[156:159], v[60:63]\n" \
  "s_waitcnt lgkmcnt(3)\n" \
  "ds_read_b64_tr_b16 v[138:139], v161 offset:4608\n" \
  "v_mfma_f32_16x16x32_bf16 v[64:67], v[128:131], v[144:147], v[64:67]\n" \
  "s_waitcnt lgkmcnt(2)\n" \
  "ds_read_b64_tr_b16 v[140:141], v163 offset:512\n" \
  "v_mfma_f32_16x16x32_bf16 v[68:71], v[132:135], v[144:147], v[68:71]\n" \
  "ds_read_b64_tr_b16 v[142:143], v163 offset:4608\n" \
  "v_mfma_f32_16x16x32_bf16 v[72:75], v[128:131], v[148:151], v[72:75]\n" \
  "s_and_b32 s55, s59, 0x3c000\n" \
  "v_add_u32 v161, s52, v162\n" \
  "v_mfma_f32_16x16x32_bf16 v[76:79], v[132:135], v[148:151], v[76:79]\n" \
  "v_readlane_b32 s50, v160, s54\n" \
  "v_mfma_f32_16x16x32_bf16 v[80:83], v[128:131], v[152:155], v[80:83]\n" \
  "v_xor_b32 v163, 32, v161\n" \
  "v_mfma_f32_16x16x32_bf16 v[84:87], v[132:135], v[152:155], v[84:87]\n" \
  "v_mfma_f32_16x16x32_bf16 v[88:91], v[128:131], v[156:159], v[88:91]\n" \
  "ds_read_b64_tr_b16 v[128:129], v161\n" \
  "v_mfma_f32_16x16x32_bf16 v[92:95], v[132:135], v[156:159], v[92:95]\n" \
  "s_waitcnt lgkmcnt(3)\n" \
  "ds_read_b64_tr_b16 v[130:131], v161 offset:4096\n" \
  "v_add_u32 v164, s55, v167\n" \
  "v_mfma_f32_16x16x32_bf16 v[96:99], v[136:139], v[144:147], v[96:99]\n" \
  "s_waitcnt lgkmcnt(2)\n" \
  "s_add_u32 s54, s54, 1\n" \
  "ds_read_b64_tr_b16 v[132:133], v163\n" \
  "v_mfma_f32_16x16x32_bf16 v[100:103], v[140:143], v[144:147], v[100:103]\n" \
  "ds_read_b128 v[144:147], v164\n" \
  "v_mfma_f32_16x16x32_bf16 v[104:107], v[136:139], v[148:151], v[104:107]\n" \
  "ds_read_b64_tr_b16 v[134:135], v163 offset:4096\n" \
  "v_mfma_f32_16x16x32_bf16 v[108:111], v[140:143], v[148:151], v[108:111]\n" \
  "ds_read_b128 v[148:151], v164 offset:1024\n" \
  "v_mfma_f32_16x16x32_bf16 v[112:115], v[136:139], v[152:155], v[112:115]\n" \
  "s_and_b32 s52, s50, 0x3c000\n" \
  "v_mfma_f32_16x16x32_bf16 v[116:119], v[140:143], v[152:155], v[116:119]\n" \
  "ds_read_b128 v[152:155], v164 offset:8192\n" \
  "v_mfma_f32_16x16x32_bf16 v[120:123], v[136:139], v[156:159], v[120:123]\n" \
  "s_cmp_lt_u32 s58, 2\n" \
  "ds_read_b64_tr_b16 v[136:137], v161 offset:512\n" \
  "v_mfma_f32_16x16x32_bf16 v[124:127], v[140:143], v[156:159], v[124:127]\n" \
  "ds_read_b128 v[156:159], v164 offset:9216\n" \
  "ds_read_b64_tr_b16 v[138:139], v161 offset:4608\n" \
  "s_cbranch_scc1 .Loloop_%=\n" \
  "ds_read_b64_tr_b16 v[140:141], v163 offset:512\n" \
  "ds_read_b64_tr_b16 v[142:143], v163 offset:4608\n" \
  "v_readlane_b32 s59, v160, 3\n" \
  "s_and_b32 s55, s59, 0x3c000\n" \
  "v_add_u32 v165, s55, v167\n" \
  "s_waitcnt lgkmcnt(0)\n" \
  "s_lshr_b32 s51, s50, 24\n" \
  "s_cbranch_scc1 .Lstub_u0_%=\n" \
  ".Lcont_u0_%=:\n" \
  "s_barrier\n" \
  "ds_read_b128 v[144:147], v164\n" \
  "ds_read_b128 v[148:151], v164 offset:1024\n" \
  "ds_read_b128 v[152:155], v164 offset:8192\n" \
  "ds_read_b128 v[156:159], v164 offset:9216\n" \
  "s_waitcnt lgkmcnt(3)\n" \
  "v_mfma_f32_16x16x32_bf16 v[0:3], v[128:131], v[144:147], v[0:3]\n" \
  "v_mfma_f32_16x16x32_bf16 v[4:7], v[132:135], v[144:147], v[4:7]\n" \
  "s_waitcnt lgkmcnt(2)\n" \
  "v_mfma_f32_16x16x32_bf16 v[8:11], v[128:131], v[148:151], v[8:11]\n" \
  "v_add_u32 v161, s52, v162\n" \
  "v_mfma_f32_16x16x32_bf16 v[12:15], v[132:135], v[148:151], v[12:15]\n" \
  "s_waitcnt lgkmcnt(1)\n" \
  "v_readlane_b32 s50, v160, 23\n" \
  "v_mfma_f32_16x16x32_bf16 v[16:19], v[128:131], v[152:155], v[16:19]\n" \
  "v_xor_b32 v163, 32, v161\n" \
  "v_mfma_f32_16x16x32_bf16 v[20:23], v[132:135], v[152:155], v[20:23]\n" \
  "s_waitcnt lgkmcnt(0)\n" \
  "v_mfma_f32_16x16x32_bf16 v[24:27], v[128:131], v[156:159], v[24:27]\n" \
  "ds_read_b64_tr_b16 v[128:129], v161\n" \
  "v_mfma_f32_16x16x32_bf16 v[28:31], v[132:135], v[156:159], v[28:31]\n" \
  "ds_read_b64_tr_b16 v[130:131], v161 offset:4096\n" \
  "v_mfma_f32_16x16x32_bf16 v[32:35], v[136:139], v[144:147], v[32:35]\n" \
  "ds_read_b64_tr_b16 v[132:133], v163\n" \
  "v_mfma_f32_16x16x32_bf16 v[36:39], v[140:143], v[144:147], v[36:39]\n" \
  "ds_read_b128 v[144:147], v164 offset:2048\n" \
  "v_mfma_f32_16x16x32_bf16 v[40:43], v[136:139], v[148:151], v[40:43]\n" \
  "ds_read_b64_tr_b16 v[134:135], v163 offset:4096\n" \
  "v_mfma_f32_16x16x32_bf16 v[44:47], v[140:143], v[148:151], v[44:47]\n" \
  "ds_read_b128 v[148:151], v164 offset:3072\n" \
  "v_mfma_f32_16x16x32_bf16 v[48:51], v[136:139], v[152:155], v[48:51]\n" \
  "s_and_b32 s52, s50, 0x3c000\n" \
  "v_mfma_f32_16x16x32_bf16 v[52:55], v[140:143], v[152:155], v[52:55]\n" \
  "ds_read_b128 v[152:155], v164 offset:10240\n" \
  "v_mfma_f32_16x16x32_bf16 v[56:59], v[136:139], v[156:159], v[56:59]\n" \
  "ds_read_b64_tr_b16 v[136:137], v161 offset:512\n" \
  "v_mfma_f32_16x16x32_bf16 v[60:63], v[140:143], v[156:159], v[60:63]\n" \
  "s_waitcnt lgkmcnt(4)\n" \
  "ds_read_b128 v[156:159], v164 offset:11264\n" \
  "ds_read_b64_tr_b16 v[138:139], v161 offset:4608\n" \
  "v_mfma_f32_16x16x32_bf16 v[0:3], v[128:131], v[144:147], v[0:3]\n" \
  "s_waitcnt lgkmcnt(5)\n" \
  "ds_read_b64_tr_b16 v[140:141], v163 offset:512\n" \
  "v_mfma_f32_16x16x32_bf16 v[4:7], v[132:135], v[144:147], v[4:7]\n" \
  "s_waitcnt lgkmcnt(5)\n" \
  "ds_read_b64_tr_b16 v[142:143], v163 offset:4608\n" \
  "v_mfma_f32_16x16x32_bf16 v[8:11], v[128:131], v[148:151], v[8:11]\n" \
  "v_add_u32 v161, s52, v162\n" \
  "v_mfma_f32_16x16x32_bf16 v[12:15], v[132:135], v[148:151], v[12:15]\n" \
  "s_waitcnt lgkmcnt(5)\n" \
  "v_readlane_b32 s50, v160, 24\n" \
  "v_mfma_f32_16x16x32_bf16 v[16:19], v[128:131], v[152:155], v[16:19]\n" \
  "v_xor_b32 v163, 32, v161\n" \
  "v_mfma_f32_16x16x32_bf16 v[20:23], v[132:135], v[152:155], v[20:23]\n" \
  "s_waitcnt lgkmcnt(3)\n" \
  "v_mfma_f32_16x16x32_bf16 v[24:27], v[128:131], v[156:159], v[24:27]\n" \
  "ds_read_b64_tr_b16 v[128:129], v161\n" \
  "v_mfma_f32_16x16x32_bf16 v[28:31], v[132:135], v[156:159], v[28:31]\n" \
  "s_waitcnt lgkmcnt(3)\n" \
  "ds_read_b64_tr_b16 v[130:131], v161 offset:4096\n" \
  "v_mfma_f32_16x16x32_bf16 v[32:35], v[136:139], v[144:147], v[32:35]\n" \
  "s_waitcnt lgkmcnt(2)\n" \
  "ds_read_b64_tr_b16 v[132:133], v163\n" \
  "v_mfma_f32_16x16x32_bf16 v[36:39], v[140:143], v[144:147], v[36:39]\n" \
  "ds_read_b128 v[144:147], v164 offset:4096\n" \
  "v_mfma_f32_16x16x32_bf16 v[40:43], v[136:139], v[148:151], v[40:43]\n" \
  "ds_read_b64_tr_b16 v[134:135], v163 offset:4096\n" \
  "v_mfma_f32_16x16x32_bf16 v[44:47], v[140:143], v[148:151], v[44:47]\n" \
  "ds_read_b128 v[148:151], v164 offset:5120\n" \
  "v_mfma_f32_16x16x32_bf16 v[48:51], v[136:139], v[152:155], v[48:51]\n" \
  "s_and_b32 s52, s50, 0x3c000\n" \
  "v_mfma_f32_16x16x32_bf16 v[52:55], v[140:143], v[152:155], v[52:55]\n" \
  "ds_read_b128 v[152:155], v164 offset:12288\n" \
  "v_mfma_f32_16x16x32_bf16 v[56:59], v[136:139], v[156:159], v[56:59]\n" \
  "ds_read_b64_tr_b16 v[136:137], v161 offset:512\n" \
  "v_mfma_f32_16x16x32_bf16 v[60:63], v[140:143], v[156:159], v[60:63]\n" \
  "s_waitcnt lgkmcnt(4)\n" \
  "ds_read_b128 v[156:159], v164 offset:13312\n" \
  "ds_read_b64_tr_b16 v[138:139], v161 offset:4608\n" \
  "v_mfma_f32_16x16x32_bf16 v[0:3], v[128:131], v[144:147], v[0:3]\n" \
  "s_waitcnt lgkmcnt(5)\n" \
  "ds_read_b64_tr_b16 v[140:141], v163 offset:512\n" \
  "v_mfma_f32_16x16x32_bf16 v[4:7], v[132:135], v[144:147], v[4:7]\n" \
  "s_waitcnt lgkmcnt(5)\n" \
  "ds_read_b64_tr_b16 v[142:143], v163 offset:4608\n" \
  "v_mfma_f32_16x16x32_bf16 v[8:11], v[128:131], v[148:151], v[8:11]\n" \
  "v_add_u32 v161, s52, v162\n" \
  "v_mfma_f32_16x16x32_bf16 v[12:15], v[132:135], v[148:151], v[12:15]\n" \
  "s_waitcnt lgkmcnt(5)\n" \
  "v_readlane_b32 s50, v160, 25\n" \
  "v_mfma_f32_16x16x32_bf16 v[16:19], v[128:131], v[152:155], v[16:19]\n" \
  "v_xor_b32 v163, 32, v161\n" \
  "v_mfma_f32_16x16x32_bf16 v[20:23], v[132:135], v[152:155], v[20:23]\n" \
  "s_waitcnt lgkmcnt(0)\n" \
  "s_barrier\n" \
  "v_mfma_f32_16x16x32_bf16 v[24:27], v[128:131], v[156:159], v[24:27]\n" \
  "ds_read_b64_tr_b16 v[128:129], v161\n" \
  "v_mfma_f32_16x16x32_bf16 v[28:31], v[132:135], v[156:159], v[28:31]\n" \
  "ds_read_b64_tr_b16 v[130:131], v161 offset:4096\n" \
  "v_mfma_f32_16x16x32_bf16 v[32:35], v[136:139], v[144:147], v[32:35]\n" \
  "ds_read_b64_tr_b16 v[132:133], v163\n" \
  "v_mfma_f32_16x16x32_bf16 v[36:39], v[140:143], v[144:147], v[36:39]\n" \
  "ds_read_b128 v[144:147], v164 offset:6144\n" \
  "v_mfma_f32_16x16x32_bf16 v[40:43], v[136:139], v[148:151], v[40:43]\n" \
  "ds_read_b64_tr_b16 v[134:135], v163 offset:4096\n" \
  "v_mfma_f32_16x16x32_bf16 v[44:47], v[140:143], v[148:151], v[44:47]\n" \
  "ds_read_b128 v[148:151], v164 offset:7168\n" \
  "v_mfma_f32_16x16x32_bf16 v[48:51], v[136:139], v[152:155], v[48:51]\n" \
  "s_and_b32 s52, s50, 0x3c000\n" \
  "v_mfma_f32_16x16x32_bf16 v[52:55], v[140:143], v[152:155], v[52:55]\n" \
  "ds_read_b128 v[152:155], v164 offset:14336\n" \
  "v_mfma_f32_16x16x32_bf16 v[56:59], v[136:139], v[156:159], v[56:59]\n" \
  "ds_read_b64_tr_b16 v[136:137], v161 offset:512\n" \
  "v_mfma_f32_16x16x32_bf16 v[60:63], v[140:143], v[156:159], v[60:63]\n" \
  "s_waitcnt lgkmcnt(4)\n" \
  "ds_read_b128 v[156:159], v164 offset:15360\n" \
  "ds_read_b64_tr_b16 v[138:139], v161 offset:4608\n" \
  "v_mfma_f32_16x16x32_bf16 v[0:3], v[128:131], v[144:147], v[0:3]\n" \
  "s_waitcnt lgkmcnt(5)\n" \
  "ds_read_b64_tr_b16 v[140:141], v163 offset:512\n" \
  "v_mfma_f32_16x16x32_bf16 v[4:7], v[132:135], v[144:147], v[4:7]\n" \
  "s_waitcnt lgkmcnt(5)\n" \
  "ds_read_b64_tr_b16 v[142:143], v163 offset:4608\n" \
  "v_mfma_f32_16x16x32_bf16 v[8:11], v[128:131], v[148:151], v[8:11]\n" \
  "v_add_u32 v161, s52, v162\n" \
  "v_mfma_f32_16x16x32_bf16 v[12:15], v[132:135], v[148:151], v[12:15]\n" \
  "s_waitcnt lgkmcnt(5)\n" \
  "v_readlane_b32 s50, v160, 26\n" \
  "v_mfma_f32_16x16x32_bf16 v[16:19], v[128:131], v[152:155], v[16:19]\n" \
  "v_xor_b32 v163, 32, v161\n" \
  "v_mfma_f32_16x16x32_bf16 v[20:23], v[132:135], v[152:155], v[20:23]\n" \
  "s_waitcnt lgkmcnt(3)\n" \
  "v_mfma_f32_16x16x32_bf16 v[24:27], v[128:131], v[156:159], v[24:27]\n" \
  "ds_read_b64_tr_b16 v[128:129], v161\n" \
  "v_mfma_f32_16x16x32_bf16 v[28:31], v[132:135], v[156:159], v[28:31]\n" \
  "s_waitcnt lgkmcnt(3)\n" \
  "ds_read_b64_tr_b16 v[130:131], v161 offset:4096\n" \
  "v_mfma_f32_16x16x32_bf16 v[32:35], v[136:139], v[144:147], v[32:35]\n" \
  "s_waitcnt lgkmcnt(2)\n" \
  "ds_read_b64_tr_b16 v[132:133], v163\n" \
  "v_mfma_f32_16x16x32_bf16 v[36:39], v[140:143], v[144:147], v[36:39]\n" \
  "ds_read_b128 v[144:147], v165\n" \
  "v_mfma_f32_16x16x32_bf16 v[40:43], v[136:139], v[148:151], v[40:43]\n" \
  "ds_read_b64_tr_b16 v[134:135], v163 offset:4096\n" \
  "v_mfma_f32_16x16x32_bf16 v[44:47], v[140:143], v[148:151], v[44:47]\n" \
  "ds_read_b128 v[148:151], v165 offset:1024\n" \
  "v_mfma_f32_16x16x32_bf16 v[48:51], v[136:139], v[152:155], v[48:51]\n" \
  "s_and_b32 s52, s50, 0x3c000\n" \
  "v_mfma_f32_16x16x32_bf16 v[52:55], v[140:143], v[152:155], v[52:55]\n" \
  "ds_read_b128 v[152:155], v165 offset:8192\n" \
  "v_mfma_f32_16x16x32_bf16 v[56:59], v[136:139], v[156:159], v[56:59]\n" \
  "ds_read_b64_tr_b16 v[136:137], v161 offset:512\n" \
  "v_mfma_f32_16x16x32_bf16 v[60:63], v[140:143], v[156:159], v[60:63]\n" \
  "s_waitcnt lgkmcnt(4)\n" \
  "ds_read_b128 v[156:159], v165 offset:9216\n" \
  "ds_read_b64_tr_b16 v[138:139], v161 offset:4608\n" \
  "v_mfma_f32_16x16x32_bf16 v[0:3], v[128:131], v[144:147], v[0:3]\n" \
  "s_waitcnt lgkmcnt(5)\n" \
  "ds_read_b64_tr_b16 v[140:141], v163 offset:512\n" \
  "v_mfma_f32_16x16x32_bf16 v[4:7], v[132:135], v[144:147], v[4:7]\n" \
  "s_waitcnt lgkmcnt(5)\n" \
  "ds_read_b64_tr_b16 v[142:143], v163 offset:4608\n" \
  "v_mfma_f32_16x16x32_bf16 v[8:11], v[128:131], v[148:151], v[8:11]\n" \
  "v_add_u32 v161, s52, v162\n" \
  "v_mfma_f32_16x16x32_bf16 v[12:15], v[132:135], v[148:151], v[12:15]\n" \
  "s_waitcnt lgkmcnt(5)\n" \
  "v_readlane_b32 s50, v160, 27\n" \
  "v_mfma_f32_16x16x32_bf16 v[16:19], v[128:131], v[152:155], v[16:19]\n" \
  "v_xor_b32 v163, 32, v161\n" \
  "v_mfma_f32_16x16x32_bf16 v[20:23], v[132:135], v[152:155], v[20:23]\n" \
  "s_waitcnt lgkmcnt(0)\n" \
  "s_barrier\n" \
  "v_mfma_f32_16x16x32_bf16 v[24:27], v[128:131], v[156:159], v[24:27]\n" \
  "ds_read_b64_tr_b16 v[128:129], v161\n" \
  "v_mfma_f32_16x16x32_bf16 v[28:31], v[132:135], v[156:159], v[28:31]\n" \
  "ds_read_b64_tr_b16 v[130:131], v161 offset:4096\n" \
  "v_mfma_f32_16x16x32_bf16 v[32:35], v[136:139], v[144:147], v[32:35]\n" \
  "ds_read_b64_tr_b16 v[132:133], v163\n" \
  "v_mfma_f32_16x16x32_bf16 v[36:39], v[140:143], v[144:147], v[36:39]\n" \
  "ds_read_b128 v[144:147], v165 offset:2048\n" \
  "v_mfma_f32_16x16x32_bf16 v[40:43], v[136:139], v[148:151], v[40:43]\n" \
  "ds_read_b64_tr_b16 v[134:135], v163 offset:4096\n" \
  "v_mfma_f32_16x16x32_bf16 v[44:47], v[140:143], v[148:151], v[44:47]\n" \
  "ds_read_b128 v[148:151], v165 offset:3072\n" \
  "v_mfma_f32_16x16x32_bf16 v[48:51], v[136:139], v[152:155], v[48:51]\n" \
  "s_and_b32 s52, s50, 0x3c000\n" \
  "v_mfma_f32_16x16x32_bf16 v[52:55], v[140:143], v[152:155], v[52:55]\n" \
  "ds_read_b128 v[152:155], v165 offset:10240\n" \
  "v_mfma_f32_16x16x32_bf16 v[56:59], v[136:139], v[156:159], v[56:59]\n" \
  "ds_read_b64_tr_b16 v[136:137], v161 offset:512\n" \
  "v_mfma_f32_16x16x32_bf16 v[60:63], v[140:143], v[156:159], v[60:63]\n" \
  "s_waitcnt lgkmcnt(4)\n" \
  "ds_read_b128 v[156:159], v165 offset:11264\n" \
  "ds_read_b64_tr_b16 v[138:139], v161 offset:4608\n" \
  "v_mfma_f32_16x16x32_bf16 v[0:3], v[128:131], v[144:147], v[0:3]\n" \
  "s_waitcnt lgkmcnt(5)\n" \
  "ds_read_b64_tr_b16 v[140:141], v163 offset:512\n" \
  "v_mfma_f32_16x16x32_bf16 v[4:7], v[132:135], v[144:147], v[4:7]\n" \
  "s_waitcnt lgkmcnt(5)\n" \
  "ds_read_b64_tr_b16 v[142:143], v163 offset:4608\n" \
  "v_mfma_f32_16x16x32_bf16 v[8:11], v[128:131], v[148:151], v[8:11]\n" \
  "v_add_u32 v161, s52, v162\n" \
  "v_mfma_f32_16x16x32_bf16 v[12:15], v[132:135], v[148:151], v[12:15]\n" \
  "s_waitcnt lgkmcnt(5)\n" \
  "v_readlane_b32 s50, v160, 28\n" \
  "v_mfma_f32_16x16x32_bf16 v[16:19], v[128:131], v[152:155], v[16:19]\n" \
  "v_xor_b32 v163, 32, v161\n" \
  "v_mfma_f32_16x16x32_bf16 v[20:23], v[132:135], v[152:155], v[20:23]\n" \
  "s_waitcnt lgkmcnt(3)\n" \
  "v_mfma_f32_16x16x32_bf16 v[24:27], v[128:131], v[156:159], v[24:27]\n" \
  "ds_read_b64_tr_b16 v[128:129], v161\n" \
  "v_mfma_f32_16x16x32_bf16 v[28:31], v[132:135], v[156:159], v[28:31]\n" \
  "s_waitcnt lgkmcnt(3)\n" \
  "ds_read_b64_tr_b16 v[130:131], v161 offset:4096\n" \
  "v_mfma_f32_16x16x32_bf16 v[32:35], v[136:139], v[144:147], v[32:35]\n" \
  "s_waitcnt lgkmcnt(2)\n" \
  "ds_read_b64_tr_b16 v[132:133], v163\n" \
  "v_mfma_f32_16x16x32_bf16 v[36:39], v[140:143], v[144:147], v[36:39]\n" \
  "ds_read_b128 v[144:147], v165 offset:4096\n" \
  "v_mfma_f32_16x16x32_bf16 v[40:43], v[136:139], v[148:151], v[40:43]\n" \
  "ds_read_b64_tr_b16 v[134:135], v163 offset:4096\n" \
  "v_mfma_f32_16x16x32_bf16 v[44:47], v[140:143], v[148:151], v[44:47]\n" \
  "ds_read_b128 v[148:151], v165 offset:5120\n" \
  "v_mfma_f32_16x16x32_bf16 v[48:51], v[136:139], v[152:155], v[48:51]\n" \
  "s_and_b32 s52, s50, 0x3c000\n" \
  "v_mfma_f32_16x16x32_bf16 v[52:55], v[140:143], v[152:155], v[52:55]\n" \
  "ds_read_b128 v[152:155], v165 offset:12288\n" \
  "v_mfma_f32_16x16x32_bf16 v[56:59], v[136:139], v[156:159], v[56:59]\n" \
  "ds_read_b64_tr_b16 v[136:137], v161 offset:512\n" \
  "v_mfma_f32_16x16x32_bf16 v[60:63], v[140:143], v[156:159], v[60:63]\n" \
  "s_waitcnt lgkmcnt(4)\n" \
  "ds_read_b128 v[156:159], v165 offset:13312\n" \
  "ds_read_b64_tr_b16 v[138:139], v161 offset:4608\n" \
  "v_mfma_f32_16x16x32_bf16 v[0:3], v[128:131], v[144:147], v[0:3]\n" \
  "s_waitcnt lgkmcnt(5)\n" \
  "ds_read_b64_tr_b16 v[140:141], v163 offset:512\n" \
  "v_mfma_f32_16x16x32_bf16 v[4:7], v[132:135], v[144:147], v[4:7]\n" \
  "s_waitcnt lgkmcnt(5)\n" \
  "ds_read_b64_tr_b16 v[142:143], v163 offset:4608\n" \
  "v_mfma_f32_16x16x32_bf16 v[8:11], v[128:131], v[148:151], v[8:11]\n" \
  "v_add_u32 v161, s52, v162\n" \
  "v_mfma_f32_16x16x32_bf16 v[12:15], v[132:135], v[148:151], v[12:15]\n" \
  "s_waitcnt lgkmcnt(5)\n" \
  "v_readlane_b32 s50, v160, 29\n" \
  "v_mfma_f32_16x16x32_bf16 v[16:19], v[128:131], v[152:155], v[16:19]\n" \
  "v_xor_b32 v163, 32, v161\n" \
  "v_mfma_f32_16x16x32_bf16 v[20:23], v[132:135], v[152:155], v[20:23]\n" \
  "s_waitcnt lgkmcnt(0)\n" \
  "s_barrier\n" \
  "v_mfma_f32_16x16x32_bf16 v[24:27], v[128:131], v[156:159], v[24:27]\n" \
  "ds_read_b64_tr_b16 v[128:129], v161\n" \
  "v_mfma_f32_16x16x32_bf16 v[28:31], v[132:135], v[156:159], v[28:31]\n" \
  "ds_read_b64_tr_b16 v[130:131], v161 offset:4096\n" \
  "v_mfma_f32_16x16x32_bf16 v[32:35], v[136:139], v[144:147], v[32:35]\n" \
  "ds_read_b64_tr_b16 v[132:133], v163\n" \
  "v_mfma_f32_16x16x32_bf16 v[36:39], v[140:143], v[144:147], v[36:39]\n" \
  "ds_read_b128 v[144:147], v165 offset:6144\n" \
  "v_mfma_f32_16x16x32_bf16 v[40:43], v[136:139], v[148:151], v[40:43]\n" \
  "ds_read_b64_tr_b16 v[134:135], v163 offset:4096\n" \
  "v_mfma_f32_16x16x32_bf16 v[44:47], v[140:143], v[148:151], v[44:47]\n" \
  "ds_read_b128 v[148:151], v165 offset:7168\n" \
  "v_mfma_f32_16x16x32_bf16 v[48:51], v[136:139], v[152:155], v[48:51]\n" \
  "s_and_b32 s52, s50, 0x3c000\n" \
  "v_mfma_f32_16x16x32_bf16 v[52:55], v[140:143], v[152:155], v[52:55]\n" \
  "ds_read_b128 v[152:155], v165 offset:14336\n" \
  "v_mfma_f32_16x16x32_bf16 v[56:59], v[136:139], v[156:159], v[56:59]\n" \
  "ds_read_b64_tr_b16 v[136:137], v161 offset:512\n" \
  "v_mfma_f32_16x16x32_bf16 v[60:63], v[140:143], v[156:159], v[60:63]\n" \
  "s_waitcnt lgkmcnt(4)\n" \
  "ds_read_b128 v[156:159], v165 offset:15360\n" \
  "ds_read_b64_tr_b16 v[138:139], v161 offset:4608\n" \
  "v_mfma_f32_16x16x32_bf16 v[0:3], v[128:131], v[144:147], v[0:3]\n" \
  "s_waitcnt lgkmcnt(5)\n" \
  "ds_read_b64_tr_b16 v[140:141], v163 offset:512\n" \
  "v_mfma_f32_16x16x32_bf16 v[4:7], v[132:135], v[144:147], v[4:7]\n" \
  "s_waitcnt lgkmcnt(5)\n" \
  "ds_read_b64_tr_b16 v[142:143], v163 offset:4608\n" \
  "v_mfma_f32_16x16x32_bf16 v[8:11], v[128:131], v[148:151], v[8:11]\n" \
  "v_add_u32 v161, s52, v162\n" \
  "v_mfma_f32_16x16x32_bf16 v[12:15], v[132:135], v[148:151], v[12:15]\n" \
  "s_waitcnt lgkmcnt(5)\n" \
  "v_readlane_b32 s50, v160, 30\n" \
  "v_mfma_f32_16x16x32_bf16 v[16:19], v[128:131], v[152:155], v[16:19]\n" \
  "v_xor_b32 v163, 32, v161\n" \
  "v_mfma_f32_16x16x32_bf16 v[20:23], v[132:135], v[152:155], v[20:23]\n" \
  "s_waitcnt lgkmcnt(3)\n" \
  "v_mfma_f32_16x16x32_bf16 v[24:27], v[128:131], v[156:159], v[24:27]\n" \
  "ds_read_b64_tr_b16 v[128:129], v161\n" \
  "v_mfma_f32_16x16x32_bf16 v[28:31], v[132:135], v[156:159], v[28:31]\n" \
  "s_waitcnt lgkmcnt(3)\n" \
  "ds_read_b64_tr_b16 v[130:131], v161 offset:4096\n" \
  "v_mfma_f32_16x16x32_bf16 v[32:35], v[136:139], v[144:147], v[32:35]\n" \
  "s_waitcnt lgkmcnt(2)\n" \
  "ds_read_b64_tr_b16 v[132:133], v163\n" \
  "v_mfma_f32_16x16x32_bf16 v[36:39], v[140:143], v[144:147], v[36:39]\n" \
  "ds_read_b128 v[144:147], v164\n" \
  "v_mfma_f32_16x16x32_bf16 v[40:43], v[136:139], v[148:151], v[40:43]\n" \
  "ds_read_b64_tr_b16 v[134:135], v163 offset:4096\n" \
  "v_mfma_f32_16x16x32_bf16 v[44:47], v[140:143], v[148:151], v[44:47]\n" \
  "ds_read_b128 v[148:151], v164 offset:1024\n" \
  "v_mfma_f32_16x16x32_bf16 v[48:51], v[136:139], v[152:155], v[48:51]\n" \
  "s_and_b32 s52, s50, 0x3c000\n" \
  "v_mfma_f32_16x16x32_bf16 v[52:55], v[140:143], v[152:155], v[52:55]\n" \
  "ds_read_b128 v[152:155], v164 offset:8192\n" \
  "v_mfma_f32_16x16x32_bf16 v[56:59], v[136:139], v[156:159], v[56:59]\n" \
  "ds_read_b64_tr_b16 v[136:137], v161 offset:512\n" \
  "v_mfma_f32_16x16x32_bf16 v[60:63], v[140:143], v[156:159], v[60:63]\n" \
  "s_waitcnt lgkmcnt(4)\n" \
  "ds_read_b128 v[156:159], v164 offset:9216\n" \
  "ds_read_b64_tr_b16 v[138:139], v161 offset:4608\n" \
  "v_mfma_f32_16x16x32_bf16 v[64:67], v[128:131], v[144:147], v[64:67]\n" \
  "s_waitcnt lgkmcnt(5)\n" \
  "ds_read_b64_tr_b16 v[140:141], v163 offset:512\n" \
  "v_mfma_f32_16x16x32_bf16 v[68:71], v[132:135], v[144:147], v[68:71]\n" \
  "s_waitcnt lgkmcnt(5)\n" \
  "ds_read_b64_tr_b16 v[142:143], v163 offset:4608\n" \
  "v_mfma_f32_16x16x32_bf16 v[72:75], v[128:131], v[148:151], v[72:75]\n" \
  "v_add_u32 v161, s52, v162\n" \
  "v_mfma_f32_16x16x32_bf16 v[76:79], v[132:135], v[148:151], v[76:79]\n" \
  "s_waitcnt lgkmcnt(5)\n" \
  "v_readlane_b32 s50, v160, 31\n" \
  "v_mfma_f32_16x16x32_bf16 v[80:83], v[128:131], v[152:155], v[80:83]\n" \
  "v_xor_b32 v163, 32, v161\n" \
  "v_mfma_f32_16x16x32_bf16 v[84:87], v[132:135], v[152:155], v[84:87]\n" \
  "s_waitcnt lgkmcnt(0)\n" \
  "s_barrier\n" \
  "v_mfma_f32_16x16x32_bf16 v[88:91], v[128:131], v[156:159], v[88:91]\n" \
  "ds_read_b64_tr_b16 v[128:129], v161\n" \
  "v_cvt_pk_bf16_f32 v0, v0, v1\n" \
  "v_mfma_f32_16x16x32_bf16 v[92:95], v[132:135], v[156:159], v[92:95]\n" \
  "ds_read_b64_tr_b16 v[130:131], v161 offset:4096\n" \
  "v_cvt_pk_bf16_f32 v1, v2, v3\n" \
  "v_mfma_f32_16x16x32_bf16 v[96:99], v[136:139], v[144:147], v[96:99]\n" \
  "ds_read_b64_tr_b16 v[132:133], v163\n" \
  "v_cvt_pk_bf16_f32 v2, v4, v5\n" \
  "v_mfma_f32_16x16x32_bf16 v[100:103], v[140:143], v[144:147], v[100:103]\n" \
  "ds_read_b128 v[144:147], v164 offset:2048\n" \
  "v_cvt_pk_bf16_f32 v3, v6, v7\n" \
  "v_mfma_f32_16x16x32_bf16 v[104:107], v[136:139], v[148:151], v[104:107]\n" \
  "ds_read_b64_tr_b16 v[134:135], v163 offset:4096\n" \
  "v_cvt_pk_bf16_f32 v8, v8, v9\n" \
  "v_mfma_f32_16x16x32_bf16 v[108:111], v[140:143], v[148:151], v[108:111]\n" \
  "ds_read_b128 v[148:151], v164 offset:3072\n" \
  "v_cvt_pk_bf16_f32 v9, v10, v11\n" \
  "v_mfma_f32_16x16x32_bf16 v[112:115], v[136:139], v[152:155], v[112:115]\n" \
  "s_and_b32 s52, s50, 0x3c000\n" \
  "v_mfma_f32_16x16x32_bf16 v[116:119], v[140:143], v[152:155], v[116:119]\n" \
  "ds_read_b128 v[152:155], v164 offset:10240\n" \
  "v_cvt_pk_bf16_f32 v10, v12, v13\n" \
  "v_mfma_f32_16x16x32_bf16 v[120:123], v[136:139], v[156:159], v[120:123]\n" \
  "ds_read_b64_tr_b16 v[136:137], v161 offset:512\n" \
  "v_cvt_pk_bf16_f32 v11, v14, v15\n" \
  "v_mfma_f32_16x16x32_bf16 v[124:127], v[140:143], v[156:159], v[124:127]\n" \
  "s_waitcnt lgkmcnt(4)\n" \
  "ds_read_b128 v[156:159], v164 offset:11264\n" \
  "ds_read_b64_tr_b16 v[138:139], v161 offset:4608\n" \
  "v_mfma_f32_16x16x32_bf16 v[64:67], v[128:131], v[144:147], v[64:67]\n" \
  "s_waitcnt lgkmcnt(5)\n" \
  "ds_read_b64_tr_b16 v[140:141], v163 offset:512\n" \
  "v_mfma_f32_16x16x32_bf16 v[68:71], v[132:135], v[144:147], v[68:71]\n" \
  "s_waitcnt lgkmcnt(5)\n" \
  "ds_read_b64_tr_b16 v[142:143], v163 offset:4608\n" \
  "v_mfma_f32_16x16x32_bf16 v[72:75], v[128:131], v[148:151], v[72:75]\n" \
  "v_add_u32 v161, s52, v162\n" \
  "v_mfma_f32_16x16x32_bf16 v[76:79], v[132:135], v[148:151], v[76:79]\n" \
  "s_waitcnt lgkmcnt(5)\n" \
  "v_readlane_b32 s50, v160, 32\n" \
  "v_mfma_f32_16x16x32_bf16 v[80:83], v[128:131], v[152:155], v[80:83]\n" \
  "v_xor_b32 v163, 32, v161\n" \
  "v_mfma_f32_16x16x32_bf16 v[84:87], v[132:135], v[152:155], v[84:87]\n" \
  "s_waitcnt lgkmcnt(3)\n" \
  "v_mfma_f32_16x16x32_bf16 v[88:91], v[128:131], v[156:159], v[88:91]\n" \
  "ds_read_b64_tr_b16 v[128:129], v161\n" \
  "v_cvt_pk_bf16_f32 v16, v16, v17\n" \
  "v_mfma_f32_16x16x32_bf16 v[92:95], v[132:135], v[156:159], v[92:95]\n" \
  "s_waitcnt lgkmcnt(3)\n" \
  "ds_read_b64_tr_b16 v[130:131], v161 offset:4096\n" \
  "v_cvt_pk_bf16_f32 v17, v18, v19\n" \
  "v_mfma_f32_16x16x32_bf16 v[96:99], v[136:139], v[144:147], v[96:99]\n" \
  "s_waitcnt lgkmcnt(2)\n" \
  "ds_read_b64_tr_b16 v[132:133], v163\n" \
  "v_cvt_pk_bf16_f32 v18, v20, v21\n" \
  "v_mfma_f32_16x16x32_bf16 v[100:103], v[140:143], v[144:147], v[100:103]\n" \
  "ds_read_b128 v[144:147], v164 offset:4096\n" \
  "v_cvt_pk_bf16_f32 v19, v22, v23\n" \
  "v_mfma_f32_16x16x32_bf16 v[104:107], v[136:139], v[148:151], v[104:107]\n" \
  "ds_read_b64_tr_b16 v[134:135], v163 offset:4096\n" \
  "v_cvt_pk_bf16_f32 v24, v24, v25\n" \
  "v_mfma_f32_16x16x32_bf16 v[108:111], v[140:143], v[148:151], v[108:111]\n" \
  "ds_read_b128 v[148:151], v164 offset:5120\n" \
  "v_cvt_pk_bf16_f32 v25, v26, v27\n" \
  "v_mfma_f32_16x16x32_bf16 v[112:115], v[136:139], v[152:155], v[112:115]\n" \
  "s_and_b32 s52, s50, 0x3c000\n" \
  "v_mfma_f32_16x16x32_bf16 v[116:119], v[140:143], v[152:155], v[116:119]\n" \
  "ds_read_b128 v[152:155], v164 offset:12288\n" \
  "v_cvt_pk_bf16_f32 v26, v28, v29\n" \
  "v_mfma_f32_16x16x32_bf16 v[120:123], v[136:139], v[156:159], v[120:123]\n" \
  "ds_read_b64_tr_b16 v[136:137], v161 offset:512\n" \
  "v_cvt_pk_bf16_f32 v27, v30, v31\n" \
  "v_mfma_f32_16x16x32_bf16 v[124:127], v[140:143], v[156:159], v[124:127]\n" \
  "s_waitcnt lgkmcnt(4)\n" \
  "ds_read_b128 v[156:159], v164 offset:13312\n" \
  "ds_read_b64_tr_b16 v[138:139], v161 offset:4608\n" \
  "v_mfma_f32_16x16x32_bf16 v[64:67], v[128:131], v[144:147], v[64:67]\n" \
  "s_waitcnt lgkmcnt(5)\n" \
  "ds_read_b64_tr_b16 v[140:141], v163 offset:512\n" \
  "v_mfma_f32_16x16x32_bf16 v[68:71], v[132:135], v[144:147], v[68:71]\n" \
  "s_waitcnt lgkmcnt(5)\n" \
  "ds_read_b64_tr_b16 v[142:143], v163 offset:4608\n" \
  "v_mfma_f32_16x16x32_bf16 v[72:75], v[128:131], v[148:151], v[72:75]\n" \
  "v_add_u32 v161, s52, v162\n" \
  "v_mfma_f32_16x16x32_bf16 v[76:79], v[132:135], v[148:151], v[76:79]\n" \
  "s_waitcnt lgkmcnt(5)\n" \
  "s_mov_b64 exec, s[32:33]\n" \
  "global_store_dwordx4 v167, v[0:3], s[46:47]" PSC "\n" \
  "s_mov_b64 exec, -1\n" \
  "v_readlane_b32 s50, v160, 33\n" \
  "v_mfma_f32_16x16x32_bf16 v[80:83], v[128:131], v[152:155], v[80:83]\n" \
  "v_xor_b32 v163, 32, v161\n" \
  "v_mfma_f32_16x16x32_bf16 v[84:87], v[132:135], v[152:155], v[84:87]\n" \
  "s_waitcnt lgkmcnt(0)\n" \
  "s_barrier\n" \
  "v_mfma_f32_16x16x32_bf16 v[88:91], v[128:131], v[156:159], v[88:91]\n" \
  "ds_read_b64_tr_b16 v[128:129], v161\n" \
  "v_cvt_pk_bf16_f32 v32, v32, v33\n" \
  "v_mfma_f32_16x16x32_bf16 v[92:95], v[132:135], v[156:159], v[92:95]\n" \
  "ds_read_b64_tr_b16 v[130:131], v161 offset:4096\n" \
  "v_cvt_pk_bf16_f32 v33, v34, v35\n" \
  "v_mfma_f32_16x16x32_bf16 v[96:99], v[136:139], v[144:147], v[96:99]\n" \
  "ds_read_b64_tr_b16 v[132:133], v163\n" \
  "v_cvt_pk_bf16_f32 v34, v36, v37\n" \
  "v_mfma_f32_16x16x32_bf16 v[100:103], v[140:143], v[144:147], v[100:103]\n" \
  "ds_read_b128 v[144:147], v164 offset:6144\n" \
  "v_cvt_pk_bf16_f32 v35, v38, v39\n" \
  "v_mfma_f32_16x16x32_bf16 v[104:107], v[136:139], v[148:151], v[104:107]\n" \
  "ds_read_b64_tr_b16 v[134:135], v163 offset:4096\n" \
  "v_cvt_pk_bf16_f32 v40, v40, v41\n" \
  "v_mfma_f32_16x16x32_bf16 v[108:111], v[140:143], v[148:151], v[108:111]\n" \
  "ds_read_b128 v[148:151], v164 offset:7168\n" \
  "v_cvt_pk_bf16_f32 v41, v42, v43\n" \
  "v_mfma_f32_16x16x32_bf16 v[112:115], v[136:139], v[152:155], v[112:115]\n" \
  "s_and_b32 s52, s50, 0x3c000\n" \
  "s_mov_b64 exec, s[32:33]\n" \
  "global_store_dwordx4 v167, v[8:11], s[46:47] offset:1024" PSC "\n" \
  "s_mov_b64 exec, -1\n" \
  "v_mfma_f32_16x16x32_bf16 v[116:119], v[140:143], v[152:155], v[116:119]\n" \
  "ds_read_b128 v[152:155], v164 offset:14336\n" \
  "v_cvt_pk_bf16_f32 v42, v44, v45\n" \
  "v_mfma_f32_16x16x32_bf16 v[120:123], v[136:139], v[156:159], v[120:123]\n" \
  "ds_read_b64_tr_b16 v[136:137], v161 offset:512\n" \
  "v_cvt_pk_bf16_f32 v43, v46, v47\n" \
  "v_mfma_f32_16x16x32_bf16 v[124:127], v[140:143], v[156:159], v[124:127]\n" \
  "s_waitcnt lgkmcnt(4)\n" \
  "ds_read_b128 v[156:159], v164 offset:15360\n" \
  "ds_read_b64_tr_b16 v[138:139], v161 offset:4608\n" \
  "v_mfma_f32_16x16x32_bf16 v[64:67], v[128:131], v[144:147], v[64:67]\n" \
  "s_waitcnt lgkmcnt(5)\n" \
  "ds_read_b64_tr_b16 v[140:141], v163 offset:512\n" \
  "v_mfma_f32_16x16x32_bf16 v[68:71], v[132:135], v[144:147], v[68:71]\n" \
  "s_waitcnt lgkmcnt(5)\n" \
  "ds_read_b64_tr_b16 v[142:143], v163 offset:4608\n" \
  "v_mfma_f32_16x16x32_bf16 v[72:75], v[128:131], v[148:151], v[72:75]\n" \
  "v_add_u32 v161, s52, v162\n" \
  "v_mfma_f32_16x16x32_bf16 v[76:79], v[132:135], v[148:151], v[76:79]\n" \
  "s_waitcnt lgkmcnt(5)\n" \
  "s_mov_b64 exec, s[34:35]\n" \
  "global_store_dwordx4 v167, v[16:19], s[46:47] offset:2048" PSC "\n" \
  "s_mov_b64 exec, -1\n" \
  "v_readlane_b32 s50, v160, 34\n" \
  "v_mfma_f32_16x16x32_bf16 v[80:83], v[128:131], v[152:155], v[80:83]\n" \
  "v_xor_b32 v163, 32, v161\n" \
  "v_mfma_f32_16x16x32_bf16 v[84:87], v[132:135], v[152:155], v[84:87]\n" \
  "s_waitcnt lgkmcnt(3)\n" \
  "v_mfma_f32_16x16x32_bf16 v[88:91], v[128:131], v[156:159], v[88:91]\n" \
  "ds_read_b64_tr_b16 v[128:129], v161\n" \
  "v_cvt_pk_bf16_f32 v48, v48, v49\n" \
  "v_mfma_f32_16x16x32_bf16 v[92:95], v[132:135], v[156:159], v[92:95]\n" \
  "s_waitcnt lgkmcnt(3)\n" \
  "ds_read_b64_tr_b16 v[130:131], v161 offset:4096\n" \
  "v_cvt_pk_bf16_f32 v49, v50, v51\n" \
  "v_mfma_f32_16x16x32_bf16 v[96:99], v[136:139], v[144:147], v[96:99]\n" \
  "s_waitcnt lgkmcnt(2)\n" \
  "ds_read_b64_tr_b16 v[132:133], v163\n" \
  "v_cvt_pk_bf16_f32 v50, v52, v53\n" \
  "v_mfma_f32_16x16x32_bf16 v[100:103], v[140:143], v[144:147], v[100:103]\n" \
  "ds_read_b128 v[144:147], v165\n" \
  "v_cvt_pk_bf16_f32 v51, v54, v55\n" \
  "v_mfma_f32_16x16x32_bf16 v[104:107], v[136:139], v[148:151], v[104:107]\n" \
  "ds_read_b64_tr_b16 v[134:135], v163 offset:4096\n" \
  "v_cvt_pk_bf16_f32 v56, v56, v57\n" \
  "v_mfma_f32_16x16x32_bf16 v[108:111], v[140:143], v[148:151], v[108:111]\n" \
  "ds_read_b128 v[148:151], v165 offset:1024\n" \
  "v_cvt_pk_bf16_f32 v57, v58, v59\n" \
  "v_mfma_f32_16x16x32_bf16 v[112:115], v[136:139], v[152:155], v[112:115]\n" \
  "s_and_b32 s52, s50, 0x3c000\n" \
  "s_mov_b64 exec, s[34:35]\n" \
  "global_store_dwordx4 v167, v[24:27], s[46:47] offset:3072" PSC "\n" \
  "s_mov_b64 exec, -1\n" \
  "s_nop 0\n" \
  "v_add_u32 v167, 4096, v167\n" \
  "v_mfma_f32_16x16x32_bf16 v[116:119], v[140:143], v[152:155], v[116:119]\n" \
  "ds_read_b128 v[152:155], v165 offset:8192\n" \
  "v_cvt_pk_bf16_f32 v58, v60, v61\n" \
  "v_mfma_f32_16x16x32_bf16 v[120:123], v[136:139], v[156:159], v[120:123]\n" \
  "ds_read_b64_tr_b16 v[136:137], v161 offset:512\n" \
  "v_cvt_pk_bf16_f32 v59, v62, v63\n" \
  "v_mfma_f32_16x16x32_bf16 v[124:127], v[140:143], v[156:159], v[124:127]\n" \
  "s_waitcnt lgkmcnt(4)\n" \
  "ds_read_b128 v[156:159], v165 offset:9216\n" \
  "ds_read_b64_tr_b16 v[138:139], v161 offset:4608\n" \
  "v_mfma_f32_16x16x32_bf16 v[64:67], v[128:131], v[144:147], v[64:67]\n" \
  "s_waitcnt lgkmcnt(5)\n" \
  "ds_read_b64_tr_b16 v[140:141], v163 offset:512\n" \
  "v_mfma_f32_16x16x32_bf16 v[68:71], v[132:135], v[144:147], v[68:71]\n" \
  "s_waitcnt lgkmcnt(5)\n" \
  "ds_read_b64_tr_b16 v[142:143], v163 offset:4608\n" \
  "v_mfma_f32_16x16x32_bf16 v[72:75], v[128:131], v[148:151], v[72:75]\n" \
  "v_add_u32 v161, s52, v162\n" \
  "v_mfma_f32_16x16x32_bf16 v[76:79], v[132:135], v[148:151], v[76:79]\n" \
  "s_waitcnt lgkmcnt(5)\n" \
  "s_mov_b64 exec, s[32:33]\n" \
  "global_store_dwordx4 v167, v[32:35], s[46:47]" PSC "\n" \
  "s_mov_b64 exec, -1\n" \
  "v_readlane_b32 s50, v160, 35\n" \
  "v_mfma_f32_16x16x32_bf16 v[80:83], v[128:131], v[152:155], v[80:83]\n" \
  "v_xor_b32 v163, 32, v161\n" \
  "v_mfma_f32_16x16x32_bf16 v[84:87], v[132:135], v[152:155], v[84:87]\n" \
  "s_waitcnt lgkmcnt(0)\n" \
  "s_barrier\n" \
  "v_mfma_f32_16x16x32_bf16 v[88:91], v[128:131], v[156:159], v[88:91]\n" \
  "ds_read_b64_tr_b16 v[128:129], v161\n" \
  "v_mfma_f32_16x16x32_bf16 v[92:95], v[132:135], v[156:159], v[92:95]\n" \
  "ds_read_b64_tr_b16 v[130:131], v161 offset:4096\n" \
  "v_mfma_f32_16x16x32_bf16 v[96:99], v[136:139], v[144:147], v[96:99]\n" \
  "ds_read_b64_tr_b16 v[132:133], v163\n" \
  "v_mfma_f32_16x16x32_bf16 v[100:103], v[140:143], v[144:147], v[100:103]\n" \
  "ds_read_b128 v[144:147], v165 offset:2048\n" \
  "v_mfma_f32_16x16x32_bf16 v[104:107], v[136:139], v[148:151], v[104:107]\n" \
  "ds_read_b64_tr_b16 v[134:135], v163 offset:4096\n" \
  "v_mfma_f32_16x16x32_bf16 v[108:111], v[140:143], v[148:151], v[108:111]\n" \
  "ds_read_b128 v[148:151], v165 offset:3072\n" \
  "v_mfma_f32_16x16x32_bf16 v[112:115], v[136:139], v[152:155], v[112:115]\n" \
  "s_and_b32 s52, s50, 0x3c000\n" \
  "s_mov_b64 exec, s[32:33]\n" \
  "global_store_dwordx4 v167, v[40:43], s[46:47] offset:1024" PSC "\n" \
  "s_mov_b64 exec, -1\n" \
  "v_mfma_f32_16x16x32_bf16 v[116:119], v[140:143], v[152:155], v[116:119]\n" \
  "ds_read_b128 v[152:155], v165 offset:10240\n" \
  "v_mfma_f32_16x16x32_bf16 v[120:123], v[136:139], v[156:159], v[120:123]\n" \
  "ds_read_b64_tr_b16 v[136:137], v161 offset:512\n" \
  "v_mfma_f32_16x16x32_bf16 v[124:127], v[140:143], v[156:159], v[124:127]\n" \
  "s_waitcnt lgkmcnt(4)\n" \
  "ds_read_b128 v[156:159], v165 offset:11264\n" \
  "ds_read_b64_tr_b16 v[138:139], v161 offset:4608\n" \
  "v_mfma_f32_16x16x32_bf16 v[64:67], v[128:131], v[144:147], v[64:67]\n" \
  "s_waitcnt lgkmcnt(5)\n" \
  "ds_read_b64_tr_b16 v[140:141], v163 offset:512\n" \
  "v_mfma_f32_16x16x32_bf16 v[68:71], v[132:135], v[144:147], v[68:71]\n" \
  "s_waitcnt lgkmcnt(5)\n" \
  "ds_read_b64_tr_b16 v[142:143], v163 offset:4608\n" \
  "v_mfma_f32_16x16x32_bf16 v[72:75], v[128:131], v[148:151], v[72:75]\n" \
  "v_add_u32 v161, s52, v162\n" \
  "v_mfma_f32_16x16x32_bf16 v[76:79], v[132:135], v[148:151], v[76:79]\n" \
  "s_waitcnt lgkmcnt(5)\n" \
  "s_mov_b64 exec, s[34:35]\n" \
  "global_store_dwordx4 v167, v[48:51], s[46:47] offset:2048" PSC "\n" \
  "s_mov_b64 exec, -1\n" \
  "v_readlane_b32 s50, v160, 36\n" \
  "v_mfma_f32_16x16x32_bf16 v[80:83], v[128:131], v[152:155], v[80:83]\n" \
  "v_xor_b32 v163, 32, v161\n" \
  "v_mfma_f32_16x16x32_bf16 v[84:87], v[132:135], v[152:155], v[84:87]\n" \
  "s_waitcnt lgkmcnt(3)\n" \
  "v_mfma_f32_16x16x32_bf16 v[88:91], v[128:131], v[156:159], v[88:91]\n" \
  "ds_read_b64_tr_b16 v[128:129], v161\n" \
  "v_mfma_f32_16x16x32_bf16 v[92:95], v[132:135], v[156:159], v[92:95]\n" \
  "s_waitcnt lgkmcnt(3)\n" \
  "ds_read_b64_tr_b16 v[130:131], v161 offset:4096\n" \
  "v_mfma_f32_16x16x32_bf16 v[96:99], v[136:139], v[144:147], v[96:99]\n" \
  "s_waitcnt lgkmcnt(2)\n" \
  "ds_read_b64_tr_b16 v[132:133], v163\n" \
  "v_mfma_f32_16x16x32_bf16 v[100:103], v[140:143], v[144:147], v[100:103]\n" \
  "ds_read_b128 v[144:147], v165 offset:4096\n" \
  "v_mfma_f32_16x16x32_bf16 v[104:107], v[136:139], v[148:151], v[104:107]\n" \
  "ds_read_b64_tr_b16 v[134:135], v163 offset:4096\n" \
  "v_mfma_f32_16x16x32_bf16 v[108:111], v[140:143], v[148:151], v[108:111]\n" \
  "ds_read_b128 v[148:151], v165 offset:5120\n" \
  "v_mfma_f32_16x16x32_bf16 v[112:115], v[136:139], v[152:155], v[112:115]\n" \
  "s_and_b32 s52, s50, 0x3c000\n" \
  "s_mov_b64 exec, s[34:35]\n" \
  "global_store_dwordx4 v167, v[56:59], s[46:47] offset:3072" PSC "\n" \
  "s_mov_b64 exec, -1\n" \
  "s_nop 0\n" \
  "v_add_u32 v167, 4096, v167\n" \
  "v_mfma_f32_16x16x32_bf16 v[116:119], v[140:143], v[152:155], v[116:119]\n" \
  "ds_read_b128 v[152:155], v165 offset:12288\n" \
  "v_mfma_f32_16x16x32_bf16 v[120:123], v[136:139], v[156:159], v[120:123]\n" \
  "ds_read_b64_tr_b16 v[136:137], v161 offset:512\n" \
  "v_mfma_f32_16x16x32_bf16 v[124:127], v[140:143], v[156:159], v[124:127]\n" \
  "s_waitcnt lgkmcnt(4)\n" \
  "ds_read_b128 v[156:159], v165 offset:13312\n" \
  "ds_read_b64_tr_b16 v[138:139], v161 offset:4608\n" \
  "v_mfma_f32_16x16x32_bf16 v[64:67], v[128:131], v[144:147], v[64:67]\n" \
  "s_waitcnt lgkmcnt(5)\n" \
  "ds_read_b64_tr_b16 v[140:141], v163 offset:512\n" \
  "v_mfma_f32_16x16x32_bf16 v[68:71], v[132:135], v[144:147], v[68:71]\n" \
  "s_waitcnt lgkmcnt(5)\n" \
  "ds_read_b64_tr_b16 v[142:143], v163 offset:4608\n" \
  "v_mfma_f32_16x16x32_bf16 v[72:75], v[128:131], v[148:151], v[72:75]\n" \
  "v_add_u32 v161, s52, v162\n" \
  "v_mfma_f32_16x16x32_bf16 v[76:79], v[132:135], v[148:151], v[76:79]\n" \
  "s_waitcnt lgkmcnt(5)\n" \
  "v_mfma_f32_16x16x32_bf16 v[80:83], v[128:131], v[152:155], v[80:83]\n" \
  "v_xor_b32 v163, 32, v161\n" \
  "v_mfma_f32_16x16x32_bf16 v[84:87], v[132:135], v[152:155], v[84:87]\n" \
  "s_waitcnt lgkmcnt(0)\n" \
  "s_barrier\n" \
  "v_mfma_f32_16x16x32_bf16 v[88:91], v[128:131], v[156:159], v[88:91]\n" \
  "ds_read_b64_tr_b16 v[128:129], v161\n" \
  "v_mfma_f32_16x16x32_bf16 v[92:95], v[132:135], v[156:159], v[92:95]\n" \
  "ds_read_b64_tr_b16 v[130:131], v161 offset:4096\n" \
  "v_mfma_f32_16x16x32_bf16 v[96:99], v[136:139], v[144:147], v[96:99]\n" \
  "ds_read_b64_tr_b16 v[132:133], v163\n" \
  "v_mfma_f32_16x16x32_bf16 v[100:103], v[140:143], v[144:147], v[100:103]\n" \
  "ds_read_b128 v[144:147], v165 offset:6144\n" \
  "v_mfma_f32_16x16x32_bf16 v[104:107], v[136:139], v[148:151], v[104:107]\n" \
  "ds_read_b64_tr_b16 v[134:135], v163 offset:4096\n" \
  "v_mfma_f32_16x16x32_bf16 v[108:111], v[140:143], v[148:151], v[108:111]\n" \
  "ds_read_b128 v[148:151], v165 offset:7168\n" \
  "v_mfma_f32_16x16x32_bf16 v[112:115], v[136:139], v[152:155], v[112:115]\n" \
  "v_mfma_f32_16x16x32_bf16 v[116:119], v[140:143], v[152:155], v[116:119]\n" \
  "ds_read_b128 v[152:155], v165 offset:14336\n" \
  "v_mfma_f32_16x16x32_bf16 v[120:123], v[136:139], v[156:159], v[120:123]\n" \
  "ds_read_b64_tr_b16 v[136:137], v161 offset:512\n" \
  "v_mfma_f32_16x16x32_bf16 v[124:127], v[140:143], v[156:159], v[124:127]\n" \
  "s_waitcnt lgkmcnt(4)\n" \
  "ds_read_b128 v[156:159], v165 offset:15360\n" \
  "ds_read_b64_tr_b16 v[138:139], v161 offset:4608\n" \
  "v_mfma_f32_16x16x32_bf16 v[64:67], v[128:131], v[144:147], v[64:67]\n" \
  "s_waitcnt lgkmcnt(5)\n" \
  "ds_read_b64_tr_b16 v[140:141], v163 offset:512\n" \
  "v_mfma_f32_16x16x32_bf16 v[68:71], v[132:135], v[144:147], v[68:71]\n" \
  "s_waitcnt lgkmcnt(5)\n" \
  "ds_read_b64_tr_b16 v[142:143], v163 offset:4608\n" \
  "v_mfma_f32_16x16x32_bf16 v[72:75], v[128:131], v[148:151], v[72:75]\n" \
  "v_mfma_f32_16x16x32_bf16 v[76:79], v[132:135], v[148:151], v[76:79]\n" \
  "s_waitcnt lgkmcnt(5)\n" \
  "v_mfma_f32_16x16x32_bf16 v[80:83], v[128:131], v[152:155], v[80:83]\n" \
  "v_mfma_f32_16x16x32_bf16 v[84:87], v[132:135], v[152:155], v[84:87]\n" \
  "s_waitcnt lgkmcnt(3)\n" \
  "v_mfma_f32_16x16x32_bf16 v[88:91], v[128:131], v[156:159], v[88:91]\n" \
  "v_mfma_f32_16x16x32_bf16 v[92:95], v[132:135], v[156:159], v[92:95]\n" \
  "s_waitcnt lgkmcnt(2)\n" \
  "v_mfma_f32_16x16x32_bf16 v[96:99], v[136:139], v[144:147], v[96:99]\n" \
  "s_waitcnt lgkmcnt(0)\n" \
  "v_mfma_f32_16x16x32_bf16 v[100:103], v[140:143], v[144:147], v[100:103]\n" \
  "v_mfma_f32_16x16x32_bf16 v[104:107], v[136:139], v[148:151], v[104:107]\n" \
  "v_cvt_pk_bf16_f32 v64, v64, v65\n" \
  "v_cvt_pk_bf16_f32 v65, v66, v67\n" \
  "v_mfma_f32_16x16x32_bf16 v[108:111], v[140:143], v[148:151], v[108:111]\n" \
  "v_cvt_pk_bf16_f32 v72, v72, v73\n" \
  "v_cvt_pk_bf16_f32 v73, v74, v75\n" \
  "v_mfma_f32_16x16x32_bf16 v[112:115], v[136:139], v[152:155], v[112:115]\n" \
  "v_mfma_f32_16x16x32_bf16 v[116:119], v[140:143], v[152:155], v[116:119]\n" \
  "v_cvt_pk_bf16_f32 v80, v80, v81\n" \
  "v_cvt_pk_bf16_f32 v81, v82, v83\n" \
  "v_mfma_f32_16x16x32_bf16 v[120:123], v[136:139], v[156:159], v[120:123]\n" \
  "v_cvt_pk_bf16_f32 v88, v88, v89\n" \
  "v_cvt_pk_bf16_f32 v89, v90, v91\n" \
  "v_mfma_f32_16x16x32_bf16 v[124:127], v[140:143], v[156:159], v[124:127]\n" \
  "v_and_b32 v164, 0x70, v167\n" \
  "v_lshlrev_b32 v164, 1, v164\n" \
  "v_readlane_b32 s50, v160, 35\n" \
  "v_cvt_pk_bf16_f32 v66, v68, v69\n" \
  "v_cvt_pk_bf16_f32 v67, v70, v71\n" \
  "s_mov_b64 exec, s[32:33]\n" \
  "global_store_dwordx4 v167, v[64:67], s[46:47]" PSC "\n" \
  "s_mov_b64 exec, -1\n" \
  "v_cvt_pk_bf16_f32 v74, v76, v77\n" \
  "v_cvt_pk_bf16_f32 v75, v78, v79\n" \
  "s_mov_b64 exec, s[32:33]\n" \
  "global_store_dwordx4 v167, v[72:75], s[46:47] offset:1024" PSC "\n" \
  "s_mov_b64 exec, -1\n" \
  "v_cvt_pk_bf16_f32 v82, v84, v85\n" \
  "v_cvt_pk_bf16_f32 v83, v86, v87\n" \
  "s_mov_b64 exec, s[34:35]\n" \
  "global_store_dwordx4 v167, v[80:83], s[46:47] offset:2048" PSC "\n" \
  "s_mov_b64 exec, -1\n" \
  "v_cvt_pk_bf16_f32 v90, v92, v93\n" \
  "v_cvt_pk_bf16_f32 v91, v94, v95\n" \
  "s_mov_b64 exec, s[34:35]\n" \
  "global_store_dwordx4 v167, v[88:91], s[46:47] offset:3072" PSC "\n" \
  "s_mov_b64 exec, -1\n" \
  "s_nop 0\n" \
  "v_add_u32 v167, 4096, v167\n" \
  "v_cvt_pk_bf16_f32 v96, v96, v97\n" \
  "v_cvt_pk_bf16_f32 v97, v98, v99\n" \
  "v_cvt_pk_bf16_f32 v98, v100, v101\n" \
  "v_cvt_pk_bf16_f32 v99, v102, v103\n" \
  "s_mov_b64 exec, s[32:33]\n" \
  "global_store_dwordx4 v167, v[96:99], s[46:47]" PSC "\n" \
  "s_mov_b64 exec, -1\n" \
  "v_cvt_pk_bf16_f32 v104, v104, v105\n" \
  "v_cvt_pk_bf16_f32 v105, v106, v107\n" \
  "v_cvt_pk_bf16_f32 v106, v108, v109\n" \
  "v_cvt_pk_bf16_f32 v107, v110, v111\n" \
  "s_mov_b64 exec, s[32:33]\n" \
  "global_store_dwordx4 v167, v[104:107], s[46:47] offset:1024" PSC "\n" \
  "s_mov_b64 exec, -1\n" \
  "v_cvt_pk_bf16_f32 v112, v112, v113\n" \
  "v_cvt_pk_bf16_f32 v113, v114, v115\n" \
  "v_cvt_pk_bf16_f32 v114, v116, v117\n" \
  "v_cvt_pk_bf16_f32 v115, v118, v119\n" \
  "s_mov_b64 exec, s[34:35]\n" \
  "global_store_dwordx4 v167, v[112:115], s[46:47] offset:2048" PSC "\n" \
  "s_mov_b64 exec, -1\n" \
  "v_cvt_pk_bf16_f32 v120, v120, v121\n" \
  "v_cvt_pk_bf16_f32 v121, v122, v123\n" \
  "v_cvt_pk_bf16_f32 v122, v124, v125\n" \
  "v_cvt_pk_bf16_f32 v123, v126, v127\n" \
  "s_mov_b64 exec, s[34:35]\n" \
  "global_store_dwordx4 v167, v[120:123], s[46:47] offset:3072" PSC "\n" \
  "s_mov_b64 exec, -1\n" \
  "s_nop 0\n" \
  "v_add_u32 v167, 4096, v167\n" \
  "s_and_b32 s52, s50, 0x3c000\n" \
  "v_and_b32 v165, 0x3f0, v167\n" \
  "v_add_u32 v165, s52, v165\n" \
  "s_not_b64 exec, s[32:33]\n" \
  "s_cbranch_scc0 .Lno0_%=\n" \
  "ds_write_b128 v165, v[0:3]\n" \
  "ds_write_b128 v165, v[8:11] offset:1024\n" \
  "ds_write_b128 v165, v[32:35] offset:2048\n" \
  "ds_write_b128 v165, v[40:43] offset:3072\n" \
  "ds_write_b128 v165, v[64:67] offset:4096\n" \
  "ds_write_b128 v165, v[72:75] offset:5120\n" \
  "ds_write_b128 v165, v[96:99] offset:6144\n" \
  "ds_write_b128 v165, v[104:107] offset:7168\n" \
  ".Lno0_%=:\n" \
  "s_not_b64 exec, s[34:35]\n" \
  "s_cbranch_scc0 .Lno1_%=\n" \
  "ds_write_b128 v165, v[16:19]\n" \
  "ds_write_b128 v165, v[24:27] offset:1024\n" \
  "ds_write_b128 v165, v[48:51] offset:2048\n" \
  "ds_write_b128 v165, v[56:59] offset:3072\n" \
  "ds_write_b128 v165, v[80:83] offset:4096\n" \
  "ds_write_b128 v165, v[88:91] offset:5120\n" \
  "ds_write_b128 v165, v[112:115] offset:6144\n" \
  "ds_write_b128 v165, v[120:123] offset:7168\n" \
  ".Lno1_%=:\n" \
  "s_mov_b64 exec, 51\n" \
  "s_waitcnt vmcnt(8)\n" \
  "global_store_dword v164, v166, s[38:39] sc1\n" \
  "s_mov_b64 exec, 68\n" \
  "s_waitcnt vmcnt(5)\n" \
  "global_store_dword v164, v166, s[38:39] sc1\n" \
  "s_mov_b64 exec, 136\n" \
  "s_waitcnt vmcnt(2)\n" \
  "global_store_dword v164, v166, s[38:39] sc1\n" \
  "s_waitcnt lgkmcnt(0)\n" \
  "s_barrier\n" \
  "s_mov_b64 exec, -1\n" \
  "s_waitcnt vmcnt(0)\n" \
  ".Lend_%=:\n" \
  "s_waitcnt lgkmcnt(0)\n" \
  "s_endpgm\n" \
  ".Lresc0_%=:\n" \
  "v_mov_b32 v167, v166\n" \
  "s_nop 1\n" \
  "v_permlane32_swap_b32 v166, v167\n" \
  "v_max3_f32 v166, v166, v167, v167\n" \
  "v_mov_b32 v167, v166\n" \
  "s_nop 1\n" \
  "v_permlane16_swap_b32 v166, v167\n" \
  "v_max3_f32 v166, v166, v167, s63\n" \
  "v_max_f32 v167, 0, v166\n" \
  "v_exp_f32 v167, -v167\n" \
  "v_sub_f32 v96, v96, v166\n" \
  "v_sub_f32 v97, v97, v166\n" \
  "v_sub_f32 v98, v98, v166\n" \
  "v_sub_f32 v99, v99, v166\n" \
  "v_sub_f32 v100, v100, v166\n" \
  "v_sub_f32 v101, v101, v166\n" \
  "v_sub_f32 v102, v102, v166\n" \
  "v_sub_f32 v103, v103, v166\n" \
  "v_sub_f32 v152, v152, v166\n" \
  "v_sub_f32 v153, v153, v166\n" \
  "v_sub_f32 v154, v154, v166\n" \
  "v_sub_f32 v155, v155, v166\n" \
  "s_cmp_lg_u32 s63, 0\n" \
  "s_cbranch_scc1 .Lfirst0_%=\n" \
  "v_mul_f32 v164, v164, v167\n" \
  "v_mul_f32 v0, v0, v167\n" \
  "v_mul_f32 v1, v1, v167\n" \
  "v_mul_f32 v2, v2, v167\n" \
  "v_mul_f32 v3, v3, v167\n" \
  "v_mul_f32 v4, v4, v167\n" \
  "v_mul_f32 v5, v5, v167\n" \
  "v_mul_f32 v6, v6, v167\n" \
  "v_mul_f32 v7, v7, v167\n" \
  "v_mul_f32 v16, v16, v167\n" \
  "v_mul_f32 v17, v17, v167\n" \
  "v_mul_f32 v18, v18, v167\n" \
  "v_mul_f32 v19, v19, v167\n" \
  "v_mul_f32 v20, v20, v167\n" \
  "v_mul_f32 v21, v21, v167\n" \
  "v_mul_f32 v22, v22, v167\n" \
  "v_mul_f32 v23, v23, v167\n" \
  "v_mul_f32 v32, v32, v167\n" \
  "v_mul_f32 v33, v33, v167\n" \
  "v_mul_f32 v34, v34, v167\n" \
  "v_mul_f32 v35, v35, v167\n" \
  "v_mul_f32 v36, v36, v167\n" \
  "v_mul_f32 v37, v37, v167\n" \
  "v_mul_f32 v38, v38, v167\n" \
  "v_mul_f32 v39, v39, v167\n" \
  "v_mul_f32 v48, v48, v167\n" \
  "v_mul_f32 v49, v49, v167\n" \
  "v_mul_f32 v50, v50, v167\n" \
  "v_mul_f32 v51, v51, v167\n" \
  "v_mul_f32 v52, v52, v167\n" \
  "v_mul_f32 v53, v53, v167\n" \
  "v_mul_f32 v54, v54, v167\n" \
  "v_mul_f32 v55, v55, v167\n" \
  ".Lfirst0_%=:\n" \
  "s_branch .Lresc_back0_%=\n" \
  ".Lresc1_%=:\n" \
  "v_mov_b32 v167, v166\n" \
  "s_nop 1\n" \
  "v_permlane32_swap_b32 v166, v167\n" \
  "v_max3_f32 v166, v166, v167, v167\n" \
  "v_mov_b32 v167, v166\n" \
  "s_nop 1\n" \
  "v_permlane16_swap_b32 v166, v167\n" \
  "v_max3_f32 v166, v166, v167, s63\n" \
  "v_max_f32 v167, 0, v166\n" \
  "v_exp_f32 v167, -v167\n" \
  "v_sub_f32 v104, v104, v166\n" \
  "v_sub_f32 v105, v105, v166\n" \
  "v_sub_f32 v106, v106, v166\n" \
  "v_sub_f32 v107, v107, v166\n" \
  "v_sub_f32 v108, v108, v166\n" \
  "v_sub_f32 v109, v109, v166\n" \
  "v_sub_f32 v110, v110, v166\n" \
  "v_sub_f32 v111, v111, v166\n" \
  "v_sub_f32 v156, v156, v166\n" \
  "v_sub_f32 v157, v157, v166\n" \
  "v_sub_f32 v158, v158, v166\n" \
  "v_sub_f32 v159, v159, v166\n" \
  "s_cmp_lg_u32 s63, 0\n" \
  "s_cbranch_scc1 .Lfirst1_%=\n" \
  "v_mul_f32 v165, v165, v167\n" \
  "v_mul_f32 v8, v8, v167\n" \
  "v_mul_f32 v9, v9, v167\n" \
  "v_mul_f32 v10, v10, v167\n" \
  "v_mul_f32 v11, v11, v167\n" \
  "v_mul_f32 v12, v12, v167\n" \
  "v_mul_f32 v13, v13, v167\n" \
  "v_mul_f32 v14, v14, v167\n" \
  "v_mul_f32 v15, v15, v167\n" \
  "v_mul_f32 v24, v24, v167\n" \
  "v_mul_f32 v25, v25, v167\n" \
  "v_mul_f32 v26, v26, v167\n" \
  "v_mul_f32 v27, v27, v167\n" \
  "v_mul_f32 v28, v28, v167\n" \
  "v_mul_f32 v29, v29, v167\n" \
  "v_mul_f32 v30, v30, v167\n" \
  "v_mul_f32 v31, v31, v167\n" \
  "v_mul_f32 v40, v40, v167\n" \
  "v_mul_f32 v41, v41, v167\n" \
  "v_mul_f32 v42, v42, v167\n" \
  "v_mul_f32 v43, v43, v167\n" \
  "v_mul_f32 v44, v44, v167\n" \
  "v_mul_f32 v45, v45, v167\n" \
  "v_mul_f32 v46, v46, v167\n" \
  "v_mul_f32 v47, v47, v167\n" \
  "v_mul_f32 v56, v56, v167\n" \
  "v_mul_f32 v57, v57, v167\n" \
  "v_mul_f32 v58, v58, v167\n" \
  "v_mul_f32 v59, v59, v167\n" \
  "v_mul_f32 v60, v60, v167\n" \
  "v_mul_f32 v61, v61, v167\n" \
  "v_mul_f32 v62, v62, v167\n" \
  "v_mul_f32 v63, v63, v167\n" \
  ".Lfirst1_%=:\n" \
  "s_mov_b32 s62, 0x41000000\n" \
  "s_mov_b32 s63, 0\n" \
  "s_branch .Lresc_back1_%=\n" \
  ".Lob1_%=:\n" \
  "s_barrier\n" \
  "s_branch .Lnb1_%=\n" \
  ".Lob3_%=:\n" \
  "s_barrier\n" \
  "s_branch .Lnb3_%=\n" \
  ".Lend1_%=:\n" \
  "s_waitcnt lgkmcnt(0)\n" \
  "s_barrier\n" \
  "s_endpgm\n" \
  ".Lstub_p0_%=:\n" \
  "s_call_b64 s[60:61], .Lidle_%=\n" \
  "s_branch .Lcont_p0_%=\n" \
  ".Lstub_a0_%=:\n" \
  "s_call_b64 s[60:61], .Lidle_%=\n" \
  "s_branch .Lcont_a0_%=\n" \
  ".Lstub_kv_%=:\n" \
  "s_call_b64 s[60:61], .Lidle_%=\n" \
  "s_branch .Lcont_kv_%=\n" \
  ".Lstub_o0p_%=:\n" \
  "s_call_b64 s[60:61], .Lidle_%=\n" \
  "s_branch .Lcont_o0p_%=\n" \
  ".Lstub_u0_%=:\n" \
  "s_call_b64 s[60:61], .Lidle_%=\n" \
  "s_branch .Lcont_u0_%=\n" \
  ".Lidle_%=:\n" \
  "s_waitcnt lgkmcnt(0)\n" \
  ".Lidle1_%=:\n" \
  "s_barrier\n" \
  "s_sub_u32 s51, s51, 1\n" \
  "s_cmp_lg_u32 s51, 0\n" \
  "s_cbranch_scc1 .Lidle1_%=\n" \
  "s_setpc_b64 s[60:61]\n"
#define LOADER_ASM \
  "s_getpc_b64 s[56:57]\n" \
  ".Lpc_%=:\n" \
  "v_readlane_b32 s53, v36, 0\n" \
  "s_add_u32 s60, s56, .Lblk0_%=-.Lpc_%=\n" \
  "s_addc_u32 s57, s57, 0\n" \
  "s_mov_b32 s59, s57\n" \
  "s_add_u32 s61, s60, .Lbar0_%=-.Lblk0_%=\n" \
  "s_mov_b32 s52, 1\n" \
  "s_mov_b32 s63, 0\n" \
  "s_lshl4_add_u32 s58, s63, s61\n" \
  "s_and_b32 s62, s53, 0xf8\n" \
  "s_lshr_b32 s55, s53, 10\n" \
  "s_lshr_b32 s63, s53, 28\n" \
  "s_and_b32 s54, s53, 0x7fff00\n" \
  "v_readlane_b32 s53, v36, s52\n" \
  "s_and_b32 s55, s55, 0x3c000\n" \
  "s_lshl4_add_u32 s56, s62, s60\n" \
  "s_add_u32 s52, s52, 1\n" \
  "s_cmp_eq_u32 s58, s61\n" \
  "s_cselect_b32 s58, s56, s58\n" \
  "s_setpc_b64 s[58:59]\n" \
  ".p2align 11\n" \
  ".Lblk0_%=:\n" \
  "s_add_u32 m0, s55, s40\n" \
  "s_xor_b32 s54, s54, s88\n" \
  "s_add_u32 s50, s72, s54\n" \
  "s_addc_u32 s51, s73, 0\n" \
  "global_load_lds_dwordx4 v52, s[50:51]" WNT "\n" \
  "s_lshl4_add_u32 s58, s63, s61\n" \
  "s_and_b32 s62, s53, 0xf8\n" \
  "s_lshr_b32 s55, s53, 10\n" \
  "s_lshr_b32 s63, s53, 28\n" \
  "s_and_b32 s54, s53, 0x7fff00\n" \
  "v_readlane_b32 s53, v36, s52\n" \
  "s_and_b32 s55, s55, 0x3c000\n" \
  "s_lshl4_add_u32 s56, s62, s60\n" \
  "s_add_u32 s52, s52, 1\n" \
  "s_cmp_eq_u32 s58, s61\n" \
  "s_cselect_b32 s58, s56, s58\n" \
  "global_load_lds_dwordx4 v53, s[50:51] offset:1024" WNT "\n" \
  "global_load_lds_dwordx4 v54, s[50:51] offset:2048" WNT "\n" \
  "global_load_lds_dwordx4 v55, s[50:51] offset:3072" WNT "\n" \
  "s_setpc_b64 s[58:59]\n" \
  ".org .Lblk0_%=+128\n" \
  ".Lblk1_%=:\n" \
  "s_add_u32 m0, s55, s40\n" \
  "s_xor_b32 s54, s54, s89\n" \
  "s_add_u32 s50, s74, s54\n" \
  "s_addc_u32 s51, s75, 0\n" \
  "global_load_lds_dwordx4 v56, s[50:51]" WNT "\n" \
  "s_lshl4_add_u32 s58, s63, s61\n" \
  "s_and_b32 s62, s53, 0xf8\n" \
  "s_lshr_b32 s55, s53, 10\n" \
  "s_lshr_b32 s63, s53, 28\n" \
  "s_and_b32 s54, s53, 0x7fff00\n" \
  "v_readlane_b32 s53, v36, s52\n" \
  "s_and_b32 s55, s55, 0x3c000\n" \
  "s_lshl4_add_u32 s56, s62, s60\n" \
  "s_add_u32 s52, s52, 1\n" \
  "s_cmp_eq_u32 s58, s61\n" \
  "s_cselect_b32 s58, s56, s58\n" \
  "global_load_lds_dwordx4 v57, s[50:51] offset:1024" WNT "\n" \
  "global_load_lds_dwordx4 v58, s[50:51] offset:2048" WNT "\n" \
  "global_load_lds_dwordx4 v59, s[50:51] offset:3072" WNT "\n" \
  "s_setpc_b64 s[58:59]\n" \
  ".org .Lblk0_%=+256\n" \
  ".Lblk2_%=:\n" \
  "s_add_u32 m0, s55, s40\n" \
  "s_xor_b32 s54, s54, s90\n" \
  "s_add_u32 s50, s76, s54\n" \
  "s_addc_u32 s51, s77, 0\n" \
  "global_load_lds_dwordx4 v60, s[50:51]" KVNT "\n" \
  "s_lshl4_add_u32 s58, s63, s61\n" \
  "s_and_b32 s62, s53, 0xf8\n" \
  "s_lshr_b32 s55, s53, 10\n" \
  "s_lshr_b32 s63, s53, 28\n" \
  "s_and_b32 s54, s53, 0x7fff00\n" \
  "v_readlane_b32 s53, v36, s52\n" \
  "s_and_b32 s55, s55, 0x3c000\n" \
  "s_lshl4_add_u32 s56, s62, s60\n" \
  "s_add_u32 s52, s52, 1\n" \
  "s_cmp_eq_u32 s58, s61\n" \
  "s_cselect_b32 s58, s56, s58\n" \
  "global_load_lds_dwordx4 v61, s[50:51] offset:1024" KVNT "\n" \
  "global_load_lds_dwordx4 v62, s[50:51] offset:2048" KVNT "\n" \
  "global_load_lds_dwordx4 v63, s[50:51] offset:3072" KVNT "\n" \
  "s_setpc_b64 s[58:59]\n" \
  ".org .Lblk0_%=+384\n" \
  ".Lblk3_%=:\n" \
  "s_add_u32 m0, s55, s40\n" \
  "s_xor_b32 s54, s54, s91\n" \
  "s_add_u32 s50, s78, s54\n" \
  "s_addc_u32 s51, s79, 0\n" \
  "global_load_lds_dwordx4 v64, s[50:51]" WNT "\n" \
  "s_lshl4_add_u32 s58, s63, s61\n" \
  "s_and_b32 s62, s53, 0xf8\n" \
  "s_lshr_b32 s55, s53, 10\n" \
  "s_lshr_b32 s63, s53, 28\n" \
  "s_and_b32 s54, s53, 0x7fff00\n" \
  "v_readlane_b32 s53, v36, s52\n" \
  "s_and_b32 s55, s55, 0x3c000\n" \
  "s_lshl4_add_u32 s56, s62, s60\n" \
  "s_add_u32 s52, s52, 1\n" \
  "s_cmp_eq_u32 s58, s61\n" \
  "s_cselect_b32 s58, s56, s58\n" \
  "global_load_lds_dwordx4 v65, s[50:51] offset:1024" WNT "\n" \
  "global_load_lds_dwordx4 v66, s[50:51] offset:2048" WNT "\n" \
  "global_load_lds_dwordx4 v67, s[50:51] offset:3072" WNT "\n" \
  "s_setpc_b64 s[58:59]\n" \
  ".org .Lblk0_%=+512\n" \
  ".Lblk4_%=:\n" \
  "v_mov_b32 v36, v37\n" \
  "v_mov_b32 v37, v38\n" \
  "s_mov_b32 s52, 1\n" \
  "s_nop 0\n" \
  "v_readlane_b32 s53, v36, 0\n" \
  "s_lshl4_add_u32 s58, s63, s61\n" \
  "s_and_b32 s62, s53, 0xf8\n" \
  "s_lshr_b32 s55, s53, 10\n" \
  "s_lshr_b32 s63, s53, 28\n" \
  "s_and_b32 s54, s53, 0x7fff00\n" \
  "v_readlane_b32 s53, v36, s52\n" \
  "s_and_b32 s55, s55, 0x3c000\n" \
  "s_lshl4_add_u32 s56, s62, s60\n" \
  "s_add_u32 s52, s52, 1\n" \
  "s_cmp_eq_u32 s58, s61\n" \
  "s_cselect_b32 s58, s56, s58\n" \
  "s_setpc_b64 s[58:59]\n" \
  ".org .Lblk0_%=+640\n" \
  ".Lblk5_%=:\n" \
  "s_lshr_b32 s62, s54, 8\n" \
  "s_and_b32 s62, s62, s92\n" \
  "s_cbranch_scc0 .Lnz_%=\n" \
  "s_add_u32 s93, s93, s62\n" \
  ".Lz_%=:\n" \
  "s_memrealtime s[70:71]\n" \
  "s_waitcnt lgkmcnt(0)\n" \
  "s_sub_u32 s71, s93, s70\n" \
  "s_cmp_le_u32 s71, s62\n" \
  "s_cbranch_scc1 .Lz_%=\n" \
  "s_cmp_ge_u32 s71, 0xfffffffa\n" \
  "s_cselect_b32 s93, s93, s70\n" \
  ".Lnz_%=:\n" \
  "s_lshl4_add_u32 s58, s63, s61\n" \
  "s_and_b32 s62, s53, 0xf8\n" \
  "s_lshr_b32 s55, s53, 10\n" \
  "s_lshr_b32 s63, s53, 28\n" \
  "s_and_b32 s54, s53, 0x7fff00\n" \
  "v_readlane_b32 s53, v36, s52\n" \
  "s_and_b32 s55, s55, 0x3c000\n" \
  "s_lshl4_add_u32 s56, s62, s60\n" \
  "s_add_u32 s52, s52, 1\n" \
  "s_cmp_eq_u32 s58, s61\n" \
  "s_cselect_b32 s58, s56, s58\n" \
  "s_setpc_b64 s[58:59]\n" \
  ".org .Lblk0_%=+768\n" \
  ".Lblk6_%=:\n" \
  "s_add_u32 s50, s84, s54\n" \
  "s_addc_u32 s51, s85, 0\n" \
  "s_mov_b64 exec, 0xff\n" \
  "global_load_dword v51, v49, s[50:51]\n" \
  "global_load_dword v51, v49, s[50:51] offset:1024\n" \
  "global_load_dword v51, v49, s[50:51] offset:2048\n" \
  "global_load_dword v51, v49, s[50:51] offset:3072\n" \
  "s_mov_b64 exec, -1\n" \
  "s_lshl4_add_u32 s58, s63, s61\n" \
  "s_and_b32 s62, s53, 0xf8\n" \
  "s_lshr_b32 s55, s53, 10\n" \
  "s_lshr_b32 s63, s53, 28\n" \
  "s_and_b32 s54, s53, 0x7fff00\n" \
  "v_readlane_b32 s53, v36, s52\n" \
  "s_and_b32 s55, s55, 0x3c000\n" \
  "s_lshl4_add_u32 s56, s62, s60\n" \
  "s_add_u32 s52, s52, 1\n" \
  "s_cmp_eq_u32 s58, s61\n" \
  "s_cselect_b32 s58, s56, s58\n" \
  "s_setpc_b64 s[58:59]\n" \
  ".org .Lblk0_%=+896\n" \
  ".Lblk7_%=:\n" \
  "s_add_u32 s50, s86, s54\n" \
  "s_addc_u32 s51, s87, 0\n" \
  "s_mov_b64 exec, 0xff\n" \
  "global_load_dword v51, v49, s[50:51]\n" \
  "global_load_dword v51, v49, s[50:51] offset:1024\n" \
  "global_load_dword v51, v49, s[50:51] offset:2048\n" \
  "global_load_dword v51, v49, s[50:51] offset:3072\n" \
  "s_mov_b64 exec, -1\n" \
  "s_lshl4_add_u32 s58, s63, s61\n" \
  "s_and_b32 s62, s53, 0xf8\n" \
  "s_lshr_b32 s55, s53, 10\n" \
  "s_lshr_b32 s63, s53, 28\n" \
  "s_and_b32 s54, s53, 0x7fff00\n" \
  "v_readlane_b32 s53, v36, s52\n" \
  "s_and_b32 s55, s55, 0x3c000\n" \
  "s_lshl4_add_u32 s56, s62, s60\n" \
  "s_add_u32 s52, s52, 1\n" \
  "s_cmp_eq_u32 s58, s61\n" \
  "s_cselect_b32 s58, s56, s58\n" \
  "s_setpc_b64 s[58:59]\n" \
  ".org .Lblk0_%=+1024\n" \
  ".Lblk8_%=:\n" \
  "s_branch .Ldone_%=\n" \
  ".org .Lblk0_%=+1152\n" \
  ".Lblk9_%=:\n" \
  "s_mov_b64 exec, 0xff\n" \
  "global_load_dword v51, v44, s[82:83] offset:2048\n" \
  "s_mov_b64 exec, 0xffff\n" \
  "global_load_dword v51, v45, s[94:95]\n" \
  "s_mov_b64 exec, 1\n" \
  "global_load_dword v51, v44, s[80:81] nt\n" \
  "global_load_dword v51, v64, s[78:79]\n" \
  "s_mov_b64 exec, -1\n" \
  "s_lshl4_add_u32 s58, s63, s61\n" \
  "s_and_b32 s62, s53, 0xf8\n" \
  "s_lshr_b32 s55, s53, 10\n" \
  "s_lshr_b32 s63, s53, 28\n" \
  "s_and_b32 s54, s53, 0x7fff00\n" \
  "v_readlane_b32 s53, v36, s52\n" \
  "s_and_b32 s55, s55, 0x3c000\n" \
  "s_lshl4_add_u32 s56, s62, s60\n" \
  "s_add_u32 s52, s52, 1\n" \
  "s_cmp_eq_u32 s58, s61\n" \
  "s_cselect_b32 s58, s56, s58\n" \
  "s_setpc_b64 s[58:59]\n" \
  ".org .Lblk0_%=+1280\n" \
  ".Lblk10_%=:\n" \
  "s_add_u32 s50, s82, s54\n" \
  "s_addc_u32 s51, s83, 0\n" \
  "s_mov_b64 exec, 0xff\n" \
  "global_load_dword v51, v49, s[50:51]\n" \
  "global_load_dword v51, v49, s[50:51] offset:1024\n" \
  "global_load_dword v51, v49, s[50:51] offset:2048\n" \
  "global_load_dword v51, v49, s[50:51] offset:3072\n" \
  "s_mov_b64 exec, -1\n" \
  "s_lshl4_add_u32 s58, s63, s61\n" \
  "s_and_b32 s62, s53, 0xf8\n" \
  "s_lshr_b32 s55, s53, 10\n" \
  "s_lshr_b32 s63, s53, 28\n" \
  "s_and_b32 s54, s53, 0x7fff00\n" \
  "v_readlane_b32 s53, v36, s52\n" \
  "s_and_b32 s55, s55, 0x3c000\n" \
  "s_lshl4_add_u32 s56, s62, s60\n" \
  "s_add_u32 s52, s52, 1\n" \
  "s_cmp_eq_u32 s58, s61\n" \
  "s_cselect_b32 s58, s56, s58\n" \
  "s_setpc_b64 s[58:59]\n" \
  ".org .Lblk0_%=+1408\n" \
  ".Lbar0_%=:\n" \
  "s_setpc_b64 s[56:57]\n" \
  ".org .Lbar0_%=+16\n" \
  "s_waitcnt vmcnt(0)\n" \
  "s_barrier\n" \
  "s_setpc_b64 s[56:57]\n" \
  ".org .Lbar0_%=+32\n" \
  "s_waitcnt vmcnt(4)\n" \
  "s_barrier\n" \
  "s_setpc_b64 s[56:57]\n" \
  ".org .Lbar0_%=+48\n" \
  "s_waitcnt vmcnt(8)\n" \
  "s_barrier\n" \
  "s_setpc_b64 s[56:57]\n" \
  ".org .Lbar0_%=+64\n" \
  "s_waitcnt vmcnt(12)\n" \
  "s_barrier\n" \
  "s_setpc_b64 s[56:57]\n" \
  ".org .Lbar0_%=+80\n" \
  "s_waitcnt vmcnt(16)\n" \
  "s_barrier\n" \
  "s_setpc_b64 s[56:57]\n" \
  ".org .Lbar0_%=+96\n" \
  "s_waitcnt vmcnt(20)\n" \
  "s_barrier\n" \
  "s_setpc_b64 s[56:57]\n" \
  ".org .Lbar0_%=+112\n" \
  "s_waitcnt vmcnt(24)\n" \
  "s_barrier\n" \
  "s_setpc_b64 s[56:57]\n" \
  ".org .Lbar0_%=+128\n" \
  "s_waitcnt vmcnt(28)\n" \
  "s_barrier\n" \
  "s_setpc_b64 s[56:57]\n" \
  ".org .Lbar0_%=+144\n" \
  "s_waitcnt vmcnt(32)\n" \
  "s_barrier\n" \
  "s_setpc_b64 s[56:57]\n" \
  ".org .Lbar0_%=+160\n" \
  "s_waitcnt vmcnt(36)\n" \
  "s_barrier\n" \
  "s_setpc_b64 s[56:57]\n" \
  ".org .Lbar0_%=+176\n" \
  "s_waitcnt vmcnt(40)\n" \
  "s_barrier\n" \
  "s_setpc_b64 s[56:57]\n" \
  ".org .Lbar0_%=+192\n" \
  "s_waitcnt vmcnt(44)\n" \
  "s_barrier\n" \
  "s_setpc_b64 s[56:57]\n" \
  ".org .Lbar0_%=+208\n" \
  "s_waitcnt vmcnt(48)\n" \
  "s_barrier\n" \
  "s_setpc_b64 s[56:57]\n" \
  ".org .Lbar0_%=+224\n" \
  "s_waitcnt vmcnt(52)\n" \
  "s_barrier\n" \
  "s_setpc_b64 s[56:57]\n" \
  ".org .Lbar0_%=+240\n" \
  "s_waitcnt vmcnt(56)\n" \
  "s_barrier\n" \
  "s_setpc_b64 s[56:57]\n" \
  ".Ldone_%=:\n" \
  "s_waitcnt vmcnt(0) lgkmcnt(0)\n"
#define COMPUTE_CLOBBERS "v0", "v1", "v2", "v3", "v4", "v5", "v6", "v7", "v8", "v9", "v10", "v11", "v12", "v13", "v14", "v15", "v16", "v17", "v18", "v19", "v20", "v21", "v22", "v23", "v24", "v25", "v26", "v27", "v28", "v29", "v30", "v31", "v32", "v33", "v34", "v35", "v36", "v37", "v38", "v39", "v40", "v41", "v42", "v43", "v44", "v45", "v46", "v47", "v48", "v49", "v50", "v51", "v52", "v53", "v54", "v55", "v56", "v57", "v58", "v59", "v60", "v61", "v62", "v63", "v64", "v65", "v66", "v67", "v68", "v69", "v70", "v71", "v72", "v73", "v74", "v75", "v76", "v77", "v78", "v79", "v80", "v81", "v82", "v83", "v84", "v85", "v86", "v87", "v88", "v89", "v90", "v91", "v92", "v93", "v94", "v95", "v96", "v97", "v98", "v99", "v100", "v101", "v102", "v103", "v104", "v105", "v106", "v107", "v108", "v109", "v110", "v111", "v112", "v113", "v114", "v115", "v116", "v117", "v118", "v119", "v120", "v121", "v122", "v123", "v124", "v125", "v126", "v127", "v128", "v129", "v130", "v131", "v132", "v133", "v134", "v135", "v136", "v137", "v138", "v139", "v140", "v141", "v142", "v143", "v145", "v146", "v147", "v148", "v149", "v150", "v151", "v152", "v153", "v154", "v155", "v156", "v157", "v158", "v159", "v160", "v163", "v164", "v165", "v166", "v167", "s50", "s51", "s52", "s53", "s54", "s55", "s56", "s57", "s58", "s59", "s60", "s61", "s62", "s63", "s64", "s65", "s66", "s67", "s68", "s69", "s70", "s71", "s72", "s73", "s74", "s75", "s76", "s77", "s78", "s79", "s80", "s81", "s82", "s83", "s84", "s85", "s86", "s87", "s88", "s89", "s90", "s91", "vcc", "scc", "memory"
#define LOADER_CLOBBERS "v36", "v37", "v38", "v51", "s93", "s50", "s51", "s52", "s53", "s54", "s55", "s56", "s57", "s58", "s59", "s60", "s61", "s62", "s63", "s64", "s65", "s66", "s67", "s68", "s69", "s70", "s71", "scc", "memory"
__global__ __launch_bounds__(768, 3) void fused_kernel(Params p) {
  extern __shared__ __attribute__((aligned(1024))) char lds[];
  typedef const volatile __attribute__((address_space(4))) u64x2* kp;
  const u64x2 kx = ((kp)__builtin_amdgcn_kernarg_segment_ptr())[0], kq = ((kp)__builtin_amdgcn_kernarg_segment_ptr())[3], ko = ((kp)__builtin_amdgcn_kernarg_segment_ptr())[4];
  __builtin_amdgcn_sched_barrier(0);
  const unsigned lb = (unsigned)(uintptr_t)(LAS char*)lds;
  const int hw = rfl(threadIdx.x >> 6), wave = hw < 4 ? hw + 8 : hw - 4;
  const int bid = blockIdx.x;
  const int g = bid & 7, br = (bid >> 3) & 1, b = bid >> 4;
  const int hsw = b & 1, rot = (b >> 1) & 3;
  const unsigned* tab = sched_tab + 512 * (br ? 1 + VARB(b) : 0);
  asm volatile("global_load_dword v160, %0, %1 offset:768" :: "v"((threadIdx.x & 63) * 4 + 320 * (wave >> 1 & 3)), "s"(tab) : "memory", "v160");

  typedef __attribute__((address_space(1))) unsigned gu32;
#define FLG ((gu32*)(p.part + (32u << 20) + (br * 16 + b) * 4096))
  if (__builtin_expect(hw < 4, 1)) {
    const int lw = wave - 8, half = lw >> 1;
    const int lane = lane_id();
    asm volatile("global_load_dword v36, %0, %1\n\tglobal_load_dword v37, %0, %1 offset:256\n\tglobal_load_dword v38, %0, %1 offset:512" :: "v"(lane * 4), "s"(tab) : "memory", "v36", "v37", "v38");
    __builtin_amdgcn_s_setprio(LPRIO);
    u32x4 oX, oW, oKV, oO;
#pragma unroll
    for (int j = 0; j < 4; ++j) {
      const unsigned cs = 128 * (j & 1) + 64 * (lane >> 5), ch = lane & 3, row = 16 * lw + 8 * (j >> 1) + ((lane >> 2) & 7), bias = 4096 - 1024 * j;
      oX[j] = row * 1024 + cs + 16 * (ch ^ ((0u - ((j & 2) | ((lane >> 4) & 1))) & 3)) + bias;
      oW[j] = (row & 31) * 8192 + 256 * half + cs + 16 * (ch ^ (j & 2)) + bias;
    }
    asm volatile("" : "+v"(oX), "+v"(oW));
    __builtin_amdgcn_sched_barrier(0);
    const uint64_t bx = (br ? kx[1] : kx[0]) + (size_t)b * 65536 - 4096, bq = (br ? kq[1] : kq[0]) + g * 1024 - 4096;
#define FIRST(o, base, dst) asm volatile("s_mov_b32 m0, %5\n\ts_nop 0\n\tglobal_load_lds_dwordx4 %0, %4\n\tglobal_load_lds_dwordx4 %1, %4 offset:1024\n\t" \
      "global_load_lds_dwordx4 %2, %4 offset:2048\n\tglobal_load_lds_dwordx4 %3, %4 offset:3072" :: "v"(o[0]), "v"(o[1]), "v"(o[2]), "v"(o[3]), "s"(base), "s"(dst) : "memory")
    FIRST(oX, bx + (rot << 8), lb + 4096 * lw);
    FIRST(oW, bq + (rot << 20 | hsw << 9), lb + 16384 + 4096 * lw);
    __builtin_amdgcn_sched_barrier(0);
    int l2 = lane, b2 = b;
    asm volatile("" : "+v"(l2), "+s"(b2));
#pragma unroll
    for (int j = 0; j < 4; ++j) {
      const unsigned cs = 128 * (j & 1) + 64 * (l2 >> 5), ch = l2 & 3, r4 = (l2 >> 4) & 1, row = (16 * lw + 8 * (j >> 1) + ((l2 >> 2) & 7)) & 31, bias = 4096 - 1024 * j;
      const unsigned nc = cs + 16 * (ch ^ ((0u - ((j & 2) | r4)) & 3)), tc = cs + 16 * (ch ^ (2 * r4));
      oO[j] = row * 1024 + 256 * half + tc + bias;
      oKV[j] = dep(row, RP, 5) * 256 + (half ? tc : nc) + bias;
    }
    const uint64_t bo = (br ? ko[1] : ko[0]) + (size_t)g * 524288 - 4096;
    const uint64_t bk = (uint64_t)(half ? p.v[br] : p.k[br]) + (size_t)((b2 * 8 + g) << (2 * br)) * 131072 - 4096;
    const uint64_t t0 = ko[0] + (size_t)g * 524288 + b2 * 32768, t1 = ko[1] + (size_t)g * 524288 + b2 * 32768;
    uint64_t pc; asm volatile("s_getpc_b64 %0" : "=s"(pc));
    asm volatile("s_waitcnt vmcnt(8)" ::: "memory");
    asm volatile(LOADER_ASM :
      : "{v49}"(lw * 4096 + l2 * 128), "{v44}"((lw * 8 + l2) * 128), "{v45}"(((l2 & 7) + (l2 & 8) * 16) * PST + 16384), "{s[82:83]}"(pc), "{s[94:95]}"(p.part + rfl(b * 8 * PST)), "{s[80:81]}"(p.out + b2 * 131072), "{v[52:55]}"(oX), "{v[56:59]}"(oW), "{v[60:63]}"(oKV), "{v[64:67]}"(oO), "{s40}"(lb + 4096 * lw),
        "{s[72:73]}"(bx), "{s[74:75]}"(bq), "{s[76:77]}"(bk), "{s[78:79]}"(bo), "{s[84:85]}"(t0), "{s[86:87]}"(t1),
        "{s88}"(rot << 8), "{s89}"(rot << 20 | hsw << 9), "{s90}"((hsw & br) << 18 | dep(lin(g | b2 << 3, KMA ^ (-br & (KMA ^ KMB))), TP, 4) << 8), "{s91}"(rot << 15 | hsw << 18), "{s92}"(rfl(lw ? 0 : -1))
      : LOADER_CLOBBERS);
    {
      gu32* const mine = FLG + (g * 4 + lw) * 8 + (lane & 7);
      const int qb = g & 1;
      const char* src = p.part + (size_t)(br * 16 + b) * 8 * PST + (g >> 1) * WST + lw * 4096 + qb * 2048;
      u32x4 v[8][2]; unsigned again;
      gu32* const theirs = FLG + ((lane & 7) * 4 + lw) * 8 + g;
      char* op = p.out + ((size_t)(b * 64 + 32 * qb + (lane & 15)) * 1024 + br * 512 + 256 * (lw >> 1) + 64 * (g >> 1) + 32 * (lw & 1)) * 2 + (lane >> 4) * 16;
      const u32x2 eye = {(lane & 2) ? 0u : 0x3f80u << (16 * (lane & 1)), (lane & 2) ? 0x3f80u << (16 * (lane & 1)) : 0u};
      const unsigned own = lb + (tab[192 + 35] >> 18 & 15) * 16384 + lw * 2048 + lane * 16;
      asm volatile("" :: "v"(op), "v"(theirs), "v"(eye), "s"(src), "v"(own));
      const unsigned want = __hip_atomic_load(mine + 512, __ATOMIC_RELAXED, __HIP_MEMORY_SCOPE_AGENT);
      unsigned f;
      asm volatile("s_barrier\n\tglobal_load_dword %0, %1, off sc1\n\ts_barrier\n\ts_waitcnt vmcnt(0)" : "=&v"(f) : "v"(mine) : "memory");
#pragma nounroll
      for (int spin = 0; spin < (1 << 16) && !__all(f == want || (lane & 7) == g); ++spin) {
        __builtin_amdgcn_s_sleep(PSLEEP);
        f = __hip_atomic_load(mine, __ATOMIC_RELAXED, __HIP_MEMORY_SCOPE_AGENT);
      }
#pragma unroll
      for (int i = 0; i < 8; ++i)
        asm volatile("s_cmp_eq_u32 %5, %6\n\ts_cbranch_scc1 .Lown_%=\n\tglobal_load_dwordx4 %0, %2, %3" RSC "\n\tglobal_load_dwordx4 %1, %2, %3 offset:1024" RSC "\n\ts_branch .Lgot_%=\n"
                     ".Lown_%=:\n\tds_read_b128 %0, %4\n\tds_read_b128 %1, %4 offset:1024\n.Lgot_%=:"
                     : "=&v"(v[i][0]), "=&v"(v[i][1]) : "v"(lane * 16), "s"(src + (size_t)i * PST), "v"(own), "s"(g), "n"(i) : "memory", "scc");
      asm volatile("global_load_dword %0, %1, off offset:2048 sc1" : "=v"(again) : "v"(theirs) : "memory");
      asm volatile("s_waitcnt vmcnt(0) lgkmcnt(0)" : "+v"(v[0][0]), "+v"(v[0][1]), "+v"(v[1][0]), "+v"(v[1][1]), "+v"(v[2][0]), "+v"(v[2][1]), "+v"(v[3][0]), "+v"(v[3][1]),
                   "+v"(v[4][0]), "+v"(v[4][1]), "+v"(v[5][0]), "+v"(v[5][1]), "+v"(v[6][0]), "+v"(v[6][1]), "+v"(v[7][0]), "+v"(v[7][1]), "+v"(again) :: "memory");
      float a[16];
      f32x4 c[4] = {};
#pragma unroll
      for (int k = 0; k < 8; ++k)
#pragma unroll
        for (int i = 0; i < 4; ++i) {
          const u32x2 d = {v[k][i >> 1][2 * (i & 1)], v[k][i >> 1][2 * (i & 1) + 1]};
          c[i] = __builtin_amdgcn_mfma_f32_4x4x4bf16_1k(__builtin_bit_cast(s16x4, eye), __builtin_bit_cast(s16x4, d), c[i], 0, 0, 0);
        }
#pragma unroll
      for (int i = 0; i < 16; ++i) a[i] = c[i >> 2][i & 3];
#pragma unroll
      for (int i = 0; i < 2; ++i) {
        unsigned x0 = cvtpk(a[8 * i + 0], a[8 * i + 1]), x1 = cvtpk(a[8 * i + 2], a[8 * i + 3]);
        unsigned y0 = cvtpk(a[8 * i + 4], a[8 * i + 5]), y1 = cvtpk(a[8 * i + 6], a[8 * i + 7]);
        auto r0 = __builtin_amdgcn_permlane32_swap(x0, y0, false, false);
        auto r1 = __builtin_amdgcn_permlane32_swap(x1, y1, false, false);
        auto s0 = __builtin_amdgcn_permlane16_swap(r0[0], r0[1], false, false);
        auto s1 = __builtin_amdgcn_permlane16_swap(r1[0], r1[1], false, false);
        u32x4 w = {s0[0], s1[0], s0[1], s1[1]};
        asm volatile("global_store_dwordx4 %0, %1, off" OSC "\n\ts_nop 1" :: "v"(op + 32768 * i), "v"(w) : "memory");
      }
      if (lane < 8) __hip_atomic_store(theirs, again, __ATOMIC_RELAXED, __HIP_MEMORY_SCOPE_AGENT);
    }
    return;
  }

  const int lane = lane_id();
#define TOUCH(reg, ptr, voff) asm volatile("global_load_dword " reg ", %0, %1" :: "v"(voff), "s"(ptr) : "memory", reg)
  if (br == 0) {
    const unsigned lin = (wave * 64 + lane) * 128;
    if (wave < 2) asm volatile("s_getpc_b64 vcc\n\tglobal_load_dword v108, %0, vcc" :: "v"(lin) : "memory", "v108", "vcc");
    TOUCH("v110", kx[1] + (size_t)b * 65536, lin);
  }
  if (wave >= 4) TOUCH("v111", (br ? kq[1] : kq[0]) + (size_t)b * 32 * 8192 + g * 1024, (8 * (wave & 3) + (lane >> 3)) * 8192 + (lane & 7) * 128);
  const int team = wave >> 2, hl = (wave >> 1) & 1, qh = wave & 1;
  const int r = lane & 15, lg = lane >> 4;
  const unsigned nb0 = lb + 2048 * (r >> 3) + 64 * (r & 7) + 16 * (lg ^ ((0 - (r >> 2)) & 3));
  const unsigned tl = lb + 64 * ((lane >> 2) & 3) + 32 * (lg & 1) + 8 * (lane & 3);
  const unsigned tb_acc = tl + 2048 * (lg >> 1) + 256 * (lg & 1), tb_wq = tl + 2048 * lg;
  const uint64_t part = (uint64_t)p.part + (size_t)((br * 16 + b) * 8 + g) * PST + wave * WST - lb;
  const uint64_t stp = 0;
#define CASM COMPUTE_ASM
  const int64_t keep0 = (int)rfl(-(2 * wave != g)), keep1 = (int)rfl(-(2 * wave + 1 != g));
  asm volatile(CASM
    :: "{v161}"(nb0), "{v162}"(tb_acc), "{v144}"(tb_wq + hl * 8192),
       "{s[32:33]}"(keep0), "{s[34:35]}"(keep1), "{s41}"(qh * 8192), "{s42}"(team), "{s44}"(rot << 11), "{s45}"(wave), "{s[46:47]}"(part), "{s[48:49]}"(stp), "{s[38:39]}"(FLG + wave * 64 + g), "{s[36:37]}"(FLG + g * 32)
    : COMPUTE_CLOBBERS);
}

extern "C" void kernel_launch(void* const* d_in, const int* in_sizes, int n_in, void* d_out, int out_size, void* d_ws, size_t ws_size, hipStream_t stream) {
  Params p;
  for (int br = 0; br < 2; ++br) {
    p.x[br] = (const char*)d_in[br * 5 + 0]; p.k[br] = (const char*)d_in[br * 5 + 1]; p.v[br] = (const char*)d_in[br * 5 + 2];
    p.wq[br] = (const char*)d_in[br * 5 + 3]; p.wo[br] = (const char*)d_in[br * 5 + 4];
  }
  p.part = (char*)d_ws; p.out = (char*)d_out;
  p.stamps = (unsigned long long*)((char*)d_ws + (64u << 20));
  (void)hipFuncSetAttribute((const void*)fused_kernel, hipFuncAttributeMaxDynamicSharedMemorySize, LDS_BYTES);
  hipLaunchKernelGGL(fused_kernel, dim3(256), dim3(768), LDS_BYTES, stream, p);
}
```
